```python
import torch

def _softmax(x, axis=-1):
    return torch.softmax(x, dim=axis)
def _gelu(x):
    return torch.nn.functional.gelu(x, approximate="tanh")
def _silu(x):
    return torch.nn.functional.silu(x)
def _sigmoid(x):
    return torch.sigmoid(x)
def _layernorm(x, axis=-1, eps=1e-5):
    m = x.mean(dim=axis, keepdim=True)
    v = x.var(dim=axis, keepdim=True, unbiased=False)
    return (x - m) / torch.sqrt(v + eps)
def _scatter_add(tgt, idx, src, axis=0):
    return tgt.clone().index_add_(axis, idx, src)
def _segment_sum(src, seg, n):
    out = torch.zeros((n,) + src.shape[1:], dtype=src.dtype)
    return out.index_add_(0, seg, src)
def _pad(x, pw):
    flat = tuple(v for pair in reversed(pw) for v in pair)
    return torch.nn.functional.pad(x, flat)

kernel_name = 'gated_mix__ssm_conv_s4d__grouped_kv_mqa__prompt'

def setup_inputs():

    _w = (lambda a: (lambda t: t.to(torch.bfloat16) if t.is_floating_point() else t)(torch.as_tensor(a)))
    def _A_setup():
        Bsz, L, D, N = 1, 2048, 640, 128
        return {
            "u": _w((lambda *s: torch.randn(*s, dtype=torch.bfloat16) * 0.05)(Bsz, L, D)),
            "log_A_real": _w((lambda *s: torch.randn(*s, dtype=torch.bfloat16) * 6.4)(D, N) * 0.5),
            "A_imag": _w((lambda *s: torch.randn(*s, dtype=torch.bfloat16) * 0.05)(D, N) * 3.14),
            "C_real": _w((lambda *s: torch.randn(*s, dtype=torch.bfloat16) * 0.05)(D, N) / N ** 0.5),
            "C_imag": _w((lambda *s: torch.randn(*s, dtype=torch.bfloat16) * 0.05)(D, N) / N ** 0.5),
            "log_dt": _w((lambda *s: torch.randn(*s, dtype=torch.bfloat16) * 0.05)(D) * 0.5 - 4.0),
            "D_skip": _w((lambda *s: torch.randn(*s, dtype=torch.bfloat16) * 0.05)(D)),
        }
    _A_in = _A_setup()

    def _B_setup():
        B, S, D, H = 1, 2048, 640, 32
        Dh = D // H
        return {
            "x": _w((lambda *s: torch.randn(*s, dtype=torch.bfloat16) * 0.05)(B, S, D)),
            "wq": _w((lambda *s: torch.randn(*s, dtype=torch.bfloat16) * 0.8)(D, H * Dh)),
            "wk": _w((lambda *s: torch.randn(*s, dtype=torch.bfloat16) * 0.8)(D, Dh)),
            "wv": _w((lambda *s: torch.randn(*s, dtype=torch.bfloat16) * 0.05)(D, Dh)),
            "wo": _w((lambda *s: torch.randn(*s, dtype=torch.bfloat16) * 0.05)(H * Dh, D)),
        }
    _B_in = _B_setup()

    return {
        "A_u": _A_in["u"],
        "A_log_A_real": _A_in["log_A_real"],
        "A_A_imag": _A_in["A_imag"],
        "A_C_real": _A_in["C_real"],
        "A_C_imag": _A_in["C_imag"],
        "A_log_dt": _A_in["log_dt"],
        "A_D_skip": _A_in["D_skip"],
        "B_x": _B_in["x"],
        "B_wq": _B_in["wq"],
        "B_wk": _B_in["wk"],
        "B_wv": _B_in["wv"],
        "B_wo": _B_in["wo"],
        "gate_w": _w((lambda *s: torch.randn(*s, dtype=torch.bfloat16) * 0.05)(640) * 16.0),
    }

def reference(A_u, A_log_A_real, A_A_imag, A_C_real, A_C_imag, A_log_dt, A_D_skip, B_x, B_wq, B_wk, B_wv, B_wo, gate_w):
    def _A_ref(u, log_A_real, A_imag, C_real, C_imag, log_dt, D_skip):
        L = u.shape[1]
        dt = torch.exp(log_dt)
        A = -torch.exp(log_A_real) + 1j * A_imag
        C = (C_real + 1j * C_imag) * dt[:, None]
        pos = (lambda x, dt: x.to(dt))(torch.arange(L), log_dt.dtype)
        P = torch.exp((A * dt[:, None])[:, :, None] * pos[None, None, :])
        K = 2.0 * torch.einsum("dn,dnl->dl", C, P).real
        L2 = 2 * L
        u_t = (lambda x, *d: x.permute(*d))(u, 0, 2, 1)
        Kf = (lambda x, *a, **k: torch.fft.rfft(x.float(), *a, **k))(K, L2)
        Uf = (lambda x, *a, **k: torch.fft.rfft(x.float(), *a, **k))(u_t, L2)
        y = (lambda x, *a, **k: torch.fft.irfft(x, *a, **k).to(torch.bfloat16))(Uf * Kf[None, :, :], L2)[:, :, :L]
        y = (lambda x, *d: x.permute(*d))(y, 0, 2, 1)
        return y + u * D_skip[None, None, :]

    def _B_ref(x, wq, wk, wv, wo):
        B, S, D = x.shape
        H = 32
        Dh = D // H
        q = (lambda x, *d: x.permute(*d))((x @ wq).reshape(B, S, H, Dh), 0, 2, 1, 3)
        k = x @ wk
        v = x @ wv
        scores = torch.einsum("bhqd,bkd->bhqk", q, k) * (Dh ** -0.5)
        p = _softmax(scores, axis=-1)
        o = torch.einsum("bhqk,bkd->bhqd", p, v)
        return (lambda x, *d: x.permute(*d))(o, 0, 2, 1, 3).reshape(B, S, H * Dh) @ wo

    x = A_u
    g = _sigmoid((x * gate_w).sum(-1, keepdims=True))
    a = _A_ref(x, A_log_A_real, A_A_imag, A_C_real, A_C_imag, A_log_dt, A_D_skip)
    b = _B_ref(x, B_wq, B_wk, B_wv, B_wo)
    return g * a + (1 - g) * b
```

```cpp
#include <hip/hip_runtime.h>
#include <hip/hip_bf16.h>

typedef __attribute__((ext_vector_type(16))) float f32x16;
typedef __attribute__((ext_vector_type(4))) float f32x4;
typedef __attribute__((ext_vector_type(2))) float f32x2;
typedef __attribute__((ext_vector_type(8))) __bf16 bf16x8;
typedef __attribute__((ext_vector_type(8))) _Float16 f16x8;
typedef __attribute__((ext_vector_type(4))) short s16x4;
typedef __attribute__((ext_vector_type(4))) unsigned u32x4;
typedef __attribute__((ext_vector_type(2))) unsigned u32x2;
typedef unsigned long long u64;
typedef unsigned short u16;
#define LAS __attribute__((address_space(3)))
#define GAS __attribute__((address_space(1)))
#define PIN(x) asm volatile("" : "+v"(x))
#define FENCE() asm volatile("" ::: "memory")
#define RENEW(x) do { unsigned t_; asm volatile("v_mov_b32 %0, %1" : "=v"(t_) : "v"(x)); x = t_; } while (0)

constexpr unsigned WS_KV_ = 262144;
constexpr unsigned WS_F1 = 4096, WS_F2 = WS_F1 + 32768, WS_F3 = WS_F2 + 32768, WS_F4 = WS_F3 + 32768, WS_F5 = WS_KV_ + 163840;
constexpr unsigned WS_KV = 262144, WS_UT = 524288, WS_AT = WS_UT + 2621440, WS_O = WS_AT + 2621440, WS_ST = WS_O + 2621440;
constexpr unsigned WS_END = WS_ST + 1048576;
constexpr unsigned L_X = 0, L_KVW = 81920, L_QX = L_KVW, L_KVO = 92160, L_KG2 = 93184, L_KAP = 159752, KAPS = 1296;
constexpr unsigned L_WO = 0, L_XB = 87040;
constexpr unsigned L_RING = 97280, SLOT = 5152, SLOTS = 4 * SLOT, L_G16 = L_RING + 40960, L_KG = L_RING + 51200;
constexpr unsigned L_CONV = L_RING + 8 * SLOT, CPS = 4160, L_U = L_CONV + 4 * CPS;
constexpr unsigned L_AT = L_CONV, L_WOD = L_CONV + 12800;
constexpr unsigned L_SYNC = 163728, L_MP = L_SYNC, L_AP = L_SYNC + 16, L_TAG = L_SYNC + 40;
constexpr unsigned L_DUMP = 163632, LDS_BYTES = 163840;
#define F4DELAY 10
constexpr unsigned SPIN_MAX = 1 << 18;

#define ASTAMP(i) ""
#define ATTN_LOOP_ASM \
  "s_waitcnt vmcnt(5)\nds_read_b128 v[152:155], v166 offset:0\nds_read_b64 v[156:157], v167 offset:0\ns_waitcnt lgkmcnt(0)\n" \
  "v_mfma_f32_32x32x16_bf16 v[64:79], v[152:155], v[128:131], 0\nv_mfma_f32_32x32x8_bf16 v[64:79], v[156:157], v[144:145], v[64:79]\ns_nop 7\ns_nop 3\n" \
  "" ASTAMP(12) "" \
  "9:\nv_mfma_f32_32x32x16_bf16 v[80:95], v[152:155], v[132:135], 0\nds_read_b32 v182, %[sy] offset:32\nv_exp_f32 v64, v64\nv_exp_f32 v65, v65\n" \
  "v_exp_f32 v66, v66\nv_exp_f32 v67, v67\nv_mfma_f32_32x32x8_bf16 v[80:95], v[156:157], v[146:147], v[80:95]\nv_exp_f32 v68, v68\nv_exp_f32 v69, v69\n" \
  "v_exp_f32 v70, v70\nv_exp_f32 v71, v71\nv_smfmac_f32_32x32x32_bf16 v[48:63], v[158:161], v[112:119], v178\nds_read_b128 v[158:161], v169 offset:0\n" \
  "v_exp_f32 v72, v72\nv_exp_f32 v73, v73\nv_exp_f32 v74, v74\nv_exp_f32 v75, v75\nv_smfmac_f32_32x32x32_bf16 v[48:63], v[162:165], v[120:127], v178\n" \
  "ds_read_b128 v[162:165], v170 offset:0\nv_exp_f32 v76, v76\nv_exp_f32 v77, v77\nv_exp_f32 v78, v78\nv_exp_f32 v79, v79\n" \
  "v_mfma_f32_32x32x16_bf16 v[96:111], v[152:155], v[136:139], 0\nv_exp_f32 v80, v80\nv_exp_f32 v81, v81\nv_exp_f32 v82, v82\nv_exp_f32 v83, v83\n" \
  "v_mfma_f32_32x32x8_bf16 v[96:111], v[156:157], v[148:149], v[96:111]\ns_waitcnt lgkmcnt(0)\nv_exp_f32 v84, v84\nv_exp_f32 v85, v85\n" \
  "v_exp_f32 v86, v86\nv_exp_f32 v87, v87\nv_smfmac_f32_32x32x32_bf16 v[0:15], v[158:161], v[64:71], v178\nv_cmp_gt_u32 vcc, %[tg], v182\n" \
  "s_cbranch_vccnz 7f\n8:\nv_exp_f32 v88, v88\nv_exp_f32 v89, v89\nv_exp_f32 v90, v90\nv_exp_f32 v91, v91\n" \
  "v_smfmac_f32_32x32x32_bf16 v[0:15], v[162:165], v[72:79], v178\ns_add_u32 %[tg], %[tg], 1\nv_exp_f32 v92, v92\nv_exp_f32 v93, v93\n" \
  "v_exp_f32 v94, v94\nv_exp_f32 v95, v95\nv_mfma_f32_32x32x16_bf16 v[112:127], v[152:155], v[140:143], 0\nds_read_b128 v[152:155], v166 offset:512\n" \
  "v_exp_f32 v96, v96\nv_exp_f32 v97, v97\nv_exp_f32 v98, v98\nv_exp_f32 v99, v99\n" \
  "v_mfma_f32_32x32x8_bf16 v[112:127], v[156:157], v[150:151], v[112:127]\nds_read_b64 v[156:157], v168 offset:0\nv_exp_f32 v100, v100\n" \
  "v_exp_f32 v101, v101\nv_exp_f32 v102, v102\nv_exp_f32 v103, v103\nv_smfmac_f32_32x32x32_bf16 v[16:31], v[158:161], v[80:87], v178\n" \
  "s_add_u32 m0, %[lb], 0\nv_exp_f32 v104, v104\nv_exp_f32 v105, v105\nv_exp_f32 v106, v106\nv_exp_f32 v107, v107\n" \
  "v_smfmac_f32_32x32x32_bf16 v[16:31], v[162:165], v[88:95], v178\ns_waitcnt lgkmcnt(0)\nv_exp_f32 v108, v108\nv_exp_f32 v109, v109\n" \
  "v_exp_f32 v110, v110\nv_exp_f32 v111, v111\nv_mfma_f32_32x32x16_bf16 v[64:79], v[152:155], v[128:131], 0\nglobal_load_lds_dwordx4 v173, s[22:23] sc1\n" \
  "v_exp_f32 v112, v112\nv_exp_f32 v113, v113\nv_exp_f32 v114, v114\nv_exp_f32 v115, v115\n" \
  "v_mfma_f32_32x32x8_bf16 v[64:79], v[156:157], v[144:145], v[64:79]\nglobal_load_lds_dwordx4 v173, s[22:23] offset:1024 sc1\nv_exp_f32 v116, v116\n" \
  "v_exp_f32 v117, v117\nv_exp_f32 v118, v118\nv_exp_f32 v119, v119\nv_smfmac_f32_32x32x32_bf16 v[32:47], v[158:161], v[96:103], v178\n" \
  "v_exp_f32 v120, v120\nv_exp_f32 v121, v121\nv_exp_f32 v122, v122\nv_exp_f32 v123, v123\n" \
  "v_smfmac_f32_32x32x32_bf16 v[32:47], v[162:165], v[104:111], v178\nv_exp_f32 v124, v124\nv_exp_f32 v125, v125\nv_exp_f32 v126, v126\n" \
  "v_exp_f32 v127, v127\nv_mfma_f32_32x32x16_bf16 v[80:95], v[152:155], v[132:135], 0\nv_exp_f32 v64, v64\nv_exp_f32 v65, v65\nv_exp_f32 v66, v66\n" \
  "v_exp_f32 v67, v67\nv_mfma_f32_32x32x8_bf16 v[80:95], v[156:157], v[146:147], v[80:95]\nv_exp_f32 v68, v68\nv_exp_f32 v69, v69\nv_exp_f32 v70, v70\n" \
  "v_exp_f32 v71, v71\nv_smfmac_f32_32x32x32_bf16 v[48:63], v[158:161], v[112:119], v178\nds_read_b128 v[158:161], v171 offset:0\nv_exp_f32 v72, v72\n" \
  "v_exp_f32 v73, v73\nv_exp_f32 v74, v74\nv_exp_f32 v75, v75\nv_smfmac_f32_32x32x32_bf16 v[48:63], v[162:165], v[120:127], v178\n" \
  "ds_read_b128 v[162:165], v172 offset:0\nv_exp_f32 v76, v76\nv_exp_f32 v77, v77\nv_exp_f32 v78, v78\nv_exp_f32 v79, v79\n" \
  "v_mfma_f32_32x32x16_bf16 v[96:111], v[152:155], v[136:139], 0\ns_add_u32 m0, %[lb], 2048\nv_exp_f32 v80, v80\nv_exp_f32 v81, v81\nv_exp_f32 v82, v82\n" \
  "v_exp_f32 v83, v83\nv_mfma_f32_32x32x8_bf16 v[96:111], v[156:157], v[148:149], v[96:111]\ns_waitcnt lgkmcnt(0)\nv_exp_f32 v84, v84\n" \
  "v_exp_f32 v85, v85\nv_exp_f32 v86, v86\nv_exp_f32 v87, v87\nv_smfmac_f32_32x32x32_bf16 v[0:15], v[158:161], v[64:71], v178\n" \
  "global_load_lds_dwordx4 v174, s[22:23] sc1\nv_exp_f32 v88, v88\nv_exp_f32 v89, v89\nv_exp_f32 v90, v90\nv_exp_f32 v91, v91\n" \
  "v_smfmac_f32_32x32x32_bf16 v[0:15], v[162:165], v[72:79], v178\ns_waitcnt vmcnt(3)\ns_add_u32 m0, %[lb], 3072\nv_exp_f32 v92, v92\n" \
  "v_exp_f32 v93, v93\nv_exp_f32 v94, v94\nv_exp_f32 v95, v95\nv_mfma_f32_32x32x16_bf16 v[112:127], v[152:155], v[140:143], 0\n" \
  "ds_read_b128 v[152:155], v166 offset:20608\nv_exp_f32 v96, v96\nv_exp_f32 v97, v97\nv_exp_f32 v98, v98\nv_exp_f32 v99, v99\n" \
  "v_mfma_f32_32x32x8_bf16 v[112:127], v[156:157], v[150:151], v[112:127]\nds_read_b64 v[156:157], v167 offset:20608\nv_exp_f32 v100, v100\n" \
  "v_exp_f32 v101, v101\nv_exp_f32 v102, v102\nv_exp_f32 v103, v103\nv_smfmac_f32_32x32x32_bf16 v[16:31], v[158:161], v[80:87], v178\n" \
  "global_load_lds_dwordx4 v175, s[22:23] sc1\nv_exp_f32 v104, v104\nv_exp_f32 v105, v105\nv_exp_f32 v106, v106\nv_exp_f32 v107, v107\n" \
  "v_smfmac_f32_32x32x32_bf16 v[16:31], v[162:165], v[88:95], v178\ns_waitcnt lgkmcnt(0)\ns_add_u32 m0, %[lb], 4096\nv_exp_f32 v108, v108\n" \
  "v_exp_f32 v109, v109\nv_exp_f32 v110, v110\nv_exp_f32 v111, v111\nv_mfma_f32_32x32x16_bf16 v[64:79], v[152:155], v[128:131], 0\n" \
  "global_load_lds_dwordx4 v176, s[22:23] sc1\nv_exp_f32 v112, v112\nv_exp_f32 v113, v113\nv_exp_f32 v114, v114\nv_exp_f32 v115, v115\n" \
  "v_mfma_f32_32x32x8_bf16 v[64:79], v[156:157], v[144:145], v[64:79]\ns_add_u32 %[o], %[o], 10240\ns_cmp_ge_u32 %[o], 163840\nv_exp_f32 v116, v116\n" \
  "v_exp_f32 v117, v117\nv_exp_f32 v118, v118\nv_exp_f32 v119, v119\nv_smfmac_f32_32x32x32_bf16 v[32:47], v[158:161], v[96:103], v178\n" \
  "s_cselect_b32 s22, 163840, 0\ns_sub_u32 %[o], %[o], s22\nv_exp_f32 v120, v120\nv_exp_f32 v121, v121\nv_exp_f32 v122, v122\nv_exp_f32 v123, v123\n" \
  "v_smfmac_f32_32x32x32_bf16 v[32:47], v[162:165], v[104:111], v178\ns_add_u32 s22, %[kl], %[o]\ns_addc_u32 s23, %[kh], 0\nv_exp_f32 v124, v124\n" \
  "v_exp_f32 v125, v125\nv_exp_f32 v126, v126\nv_exp_f32 v127, v127\nv_mfma_f32_32x32x16_bf16 v[80:95], v[152:155], v[132:135], 0\nv_exp_f32 v64, v64\n" \
  "v_exp_f32 v65, v65\nv_exp_f32 v66, v66\nv_exp_f32 v67, v67\nv_mfma_f32_32x32x8_bf16 v[80:95], v[156:157], v[146:147], v[80:95]\nv_exp_f32 v68, v68\n" \
  "v_exp_f32 v69, v69\nv_exp_f32 v70, v70\nv_exp_f32 v71, v71\nv_smfmac_f32_32x32x32_bf16 v[48:63], v[158:161], v[112:119], v178\n" \
  "ds_read_b128 v[158:161], v169 offset:20608\nv_exp_f32 v72, v72\nv_exp_f32 v73, v73\nv_exp_f32 v74, v74\nv_exp_f32 v75, v75\n" \
  "v_smfmac_f32_32x32x32_bf16 v[48:63], v[162:165], v[120:127], v178\nds_read_b128 v[162:165], v170 offset:20608\nv_exp_f32 v76, v76\n" \
  "v_exp_f32 v77, v77\nv_exp_f32 v78, v78\nv_exp_f32 v79, v79\nv_mfma_f32_32x32x16_bf16 v[96:111], v[152:155], v[136:139], 0\nv_exp_f32 v80, v80\n" \
  "v_exp_f32 v81, v81\nv_exp_f32 v82, v82\nv_exp_f32 v83, v83\nv_mfma_f32_32x32x8_bf16 v[96:111], v[156:157], v[148:149], v[96:111]\n" \
  "s_waitcnt lgkmcnt(0)\nv_exp_f32 v84, v84\nv_exp_f32 v85, v85\nv_exp_f32 v86, v86\nv_exp_f32 v87, v87\n" \
  "v_smfmac_f32_32x32x32_bf16 v[0:15], v[158:161], v[64:71], v178\nv_exp_f32 v88, v88\nv_exp_f32 v89, v89\nv_exp_f32 v90, v90\nv_exp_f32 v91, v91\n" \
  "v_smfmac_f32_32x32x32_bf16 v[0:15], v[162:165], v[72:79], v178\nv_exp_f32 v92, v92\nv_exp_f32 v93, v93\nv_exp_f32 v94, v94\nv_exp_f32 v95, v95\n" \
  "v_mfma_f32_32x32x16_bf16 v[112:127], v[152:155], v[140:143], 0\nds_read_b128 v[152:155], v166 offset:21120\nv_exp_f32 v96, v96\nv_exp_f32 v97, v97\n" \
  "v_exp_f32 v98, v98\nv_exp_f32 v99, v99\nv_mfma_f32_32x32x8_bf16 v[112:127], v[156:157], v[150:151], v[112:127]\n" \
  "ds_read_b64 v[156:157], v168 offset:20608\nv_exp_f32 v100, v100\nv_exp_f32 v101, v101\nv_exp_f32 v102, v102\nv_exp_f32 v103, v103\n" \
  "v_smfmac_f32_32x32x32_bf16 v[16:31], v[158:161], v[80:87], v178\ns_add_u32 m0, %[lb], 20608\nv_exp_f32 v104, v104\nv_exp_f32 v105, v105\n" \
  "v_exp_f32 v106, v106\nv_exp_f32 v107, v107\nv_smfmac_f32_32x32x32_bf16 v[16:31], v[162:165], v[88:95], v178\ns_waitcnt lgkmcnt(0)\n" \
  "v_exp_f32 v108, v108\nv_exp_f32 v109, v109\nv_exp_f32 v110, v110\nv_exp_f32 v111, v111\nv_mfma_f32_32x32x16_bf16 v[64:79], v[152:155], v[128:131], 0\n" \
  "global_load_lds_dwordx4 v173, s[22:23] sc1\nv_exp_f32 v112, v112\nv_exp_f32 v113, v113\nv_exp_f32 v114, v114\nv_exp_f32 v115, v115\n" \
  "v_mfma_f32_32x32x8_bf16 v[64:79], v[156:157], v[144:145], v[64:79]\nglobal_load_lds_dwordx4 v173, s[22:23] offset:1024 sc1\nv_exp_f32 v116, v116\n" \
  "v_exp_f32 v117, v117\nv_exp_f32 v118, v118\nv_exp_f32 v119, v119\nv_smfmac_f32_32x32x32_bf16 v[32:47], v[158:161], v[96:103], v178\n" \
  "v_exp_f32 v120, v120\nv_exp_f32 v121, v121\nv_exp_f32 v122, v122\nv_exp_f32 v123, v123\n" \
  "v_smfmac_f32_32x32x32_bf16 v[32:47], v[162:165], v[104:111], v178\nv_exp_f32 v124, v124\nv_exp_f32 v125, v125\nv_exp_f32 v126, v126\n" \
  "v_exp_f32 v127, v127\nv_mfma_f32_32x32x16_bf16 v[80:95], v[152:155], v[132:135], 0\nv_exp_f32 v64, v64\nv_exp_f32 v65, v65\nv_exp_f32 v66, v66\n" \
  "v_exp_f32 v67, v67\nv_mfma_f32_32x32x8_bf16 v[80:95], v[156:157], v[146:147], v[80:95]\nv_exp_f32 v68, v68\nv_exp_f32 v69, v69\nv_exp_f32 v70, v70\n" \
  "v_exp_f32 v71, v71\nv_smfmac_f32_32x32x32_bf16 v[48:63], v[158:161], v[112:119], v178\nds_read_b128 v[158:161], v171 offset:20608\n" \
  "v_exp_f32 v72, v72\nv_exp_f32 v73, v73\nv_exp_f32 v74, v74\nv_exp_f32 v75, v75\nv_smfmac_f32_32x32x32_bf16 v[48:63], v[162:165], v[120:127], v178\n" \
  "ds_read_b128 v[162:165], v172 offset:20608\nv_exp_f32 v76, v76\nv_exp_f32 v77, v77\nv_exp_f32 v78, v78\nv_exp_f32 v79, v79\n" \
  "v_mfma_f32_32x32x16_bf16 v[96:111], v[152:155], v[136:139], 0\ns_add_u32 m0, %[lb], 22656\nv_exp_f32 v80, v80\nv_exp_f32 v81, v81\n" \
  "v_exp_f32 v82, v82\nv_exp_f32 v83, v83\nv_mfma_f32_32x32x8_bf16 v[96:111], v[156:157], v[148:149], v[96:111]\ns_waitcnt lgkmcnt(0)\n" \
  "v_exp_f32 v84, v84\nv_exp_f32 v85, v85\nv_exp_f32 v86, v86\nv_exp_f32 v87, v87\nv_smfmac_f32_32x32x32_bf16 v[0:15], v[158:161], v[64:71], v178\n" \
  "global_load_lds_dwordx4 v174, s[22:23] sc1\nv_exp_f32 v88, v88\nv_exp_f32 v89, v89\nv_exp_f32 v90, v90\nv_exp_f32 v91, v91\n" \
  "v_smfmac_f32_32x32x32_bf16 v[0:15], v[162:165], v[72:79], v178\ns_waitcnt vmcnt(3)\ns_add_u32 m0, %[lb], 23680\nv_exp_f32 v92, v92\n" \
  "v_exp_f32 v93, v93\nv_exp_f32 v94, v94\nv_exp_f32 v95, v95\nv_mfma_f32_32x32x16_bf16 v[112:127], v[152:155], v[140:143], 0\n" \
  "ds_read_b128 v[152:155], v166 offset:0\nv_exp_f32 v96, v96\nv_exp_f32 v97, v97\nv_exp_f32 v98, v98\nv_exp_f32 v99, v99\n" \
  "v_mfma_f32_32x32x8_bf16 v[112:127], v[156:157], v[150:151], v[112:127]\nds_read_b64 v[156:157], v167 offset:0\nv_exp_f32 v100, v100\n" \
  "v_exp_f32 v101, v101\nv_exp_f32 v102, v102\nv_exp_f32 v103, v103\nv_smfmac_f32_32x32x32_bf16 v[16:31], v[158:161], v[80:87], v178\n" \
  "global_load_lds_dwordx4 v175, s[22:23] sc1\nv_exp_f32 v104, v104\nv_exp_f32 v105, v105\nv_exp_f32 v106, v106\nv_exp_f32 v107, v107\n" \
  "v_smfmac_f32_32x32x32_bf16 v[16:31], v[162:165], v[88:95], v178\ns_waitcnt lgkmcnt(0)\ns_add_u32 m0, %[lb], 24704\nv_exp_f32 v108, v108\n" \
  "v_exp_f32 v109, v109\nv_exp_f32 v110, v110\nv_exp_f32 v111, v111\nv_mfma_f32_32x32x16_bf16 v[64:79], v[152:155], v[128:131], 0\n" \
  "global_load_lds_dwordx4 v176, s[22:23] sc1\nv_exp_f32 v112, v112\nv_exp_f32 v113, v113\nv_exp_f32 v114, v114\nv_exp_f32 v115, v115\n" \
  "v_mfma_f32_32x32x8_bf16 v[64:79], v[156:157], v[144:145], v[64:79]\ns_add_u32 %[o], %[o], 10240\ns_cmp_ge_u32 %[o], 163840\nv_exp_f32 v116, v116\n" \
  "v_exp_f32 v117, v117\nv_exp_f32 v118, v118\nv_exp_f32 v119, v119\nv_smfmac_f32_32x32x32_bf16 v[32:47], v[158:161], v[96:103], v178\n" \
  "s_cselect_b32 s22, 163840, 0\ns_sub_u32 %[o], %[o], s22\nv_exp_f32 v120, v120\nv_exp_f32 v121, v121\nv_exp_f32 v122, v122\nv_exp_f32 v123, v123\n" \
  "v_smfmac_f32_32x32x32_bf16 v[32:47], v[162:165], v[104:111], v178\ns_add_u32 s22, %[kl], %[o]\ns_addc_u32 s23, %[kh], 0\nv_exp_f32 v124, v124\n" \
  "v_exp_f32 v125, v125\nv_exp_f32 v126, v126\nv_exp_f32 v127, v127\ns_sub_u32 %[it], %[it], 1\ns_cmp_lg_u32 %[it], 0\ns_cbranch_scc1 9b\n" \
  "v_mfma_f32_32x32x16_bf16 v[80:95], v[152:155], v[132:135], 0\nv_exp_f32 v64, v64\nv_exp_f32 v65, v65\nv_exp_f32 v66, v66\nv_exp_f32 v67, v67\n" \
  "v_mfma_f32_32x32x8_bf16 v[80:95], v[156:157], v[146:147], v[80:95]\nv_exp_f32 v68, v68\nv_exp_f32 v69, v69\nv_exp_f32 v70, v70\nv_exp_f32 v71, v71\n" \
  "v_smfmac_f32_32x32x32_bf16 v[48:63], v[158:161], v[112:119], v178\nds_read_b128 v[158:161], v169 offset:0\nv_exp_f32 v72, v72\nv_exp_f32 v73, v73\n" \
  "v_exp_f32 v74, v74\nv_exp_f32 v75, v75\nv_smfmac_f32_32x32x32_bf16 v[48:63], v[162:165], v[120:127], v178\nds_read_b128 v[162:165], v170 offset:0\n" \
  "v_exp_f32 v76, v76\nv_exp_f32 v77, v77\nv_exp_f32 v78, v78\nv_exp_f32 v79, v79\nv_mfma_f32_32x32x16_bf16 v[96:111], v[152:155], v[136:139], 0\n" \
  "v_exp_f32 v80, v80\nv_exp_f32 v81, v81\nv_exp_f32 v82, v82\nv_exp_f32 v83, v83\nv_mfma_f32_32x32x8_bf16 v[96:111], v[156:157], v[148:149], v[96:111]\n" \
  "s_waitcnt lgkmcnt(0)\nv_exp_f32 v84, v84\nv_exp_f32 v85, v85\nv_exp_f32 v86, v86\nv_exp_f32 v87, v87\n" \
  "v_smfmac_f32_32x32x32_bf16 v[0:15], v[158:161], v[64:71], v178\nv_exp_f32 v88, v88\nv_exp_f32 v89, v89\nv_exp_f32 v90, v90\nv_exp_f32 v91, v91\n" \
  "v_smfmac_f32_32x32x32_bf16 v[0:15], v[162:165], v[72:79], v178\nv_exp_f32 v92, v92\nv_exp_f32 v93, v93\nv_exp_f32 v94, v94\nv_exp_f32 v95, v95\n" \
  "v_mfma_f32_32x32x16_bf16 v[112:127], v[152:155], v[140:143], 0\nds_read_b128 v[152:155], v166 offset:512\nv_exp_f32 v96, v96\nv_exp_f32 v97, v97\n" \
  "v_exp_f32 v98, v98\nv_exp_f32 v99, v99\nv_mfma_f32_32x32x8_bf16 v[112:127], v[156:157], v[150:151], v[112:127]\n" \
  "ds_read_b64 v[156:157], v168 offset:0\nv_exp_f32 v100, v100\nv_exp_f32 v101, v101\nv_exp_f32 v102, v102\nv_exp_f32 v103, v103\n" \
  "v_smfmac_f32_32x32x32_bf16 v[16:31], v[158:161], v[80:87], v178\nv_exp_f32 v104, v104\nv_exp_f32 v105, v105\nv_exp_f32 v106, v106\n" \
  "v_exp_f32 v107, v107\nv_smfmac_f32_32x32x32_bf16 v[16:31], v[162:165], v[88:95], v178\ns_waitcnt lgkmcnt(0)\nv_exp_f32 v108, v108\n" \
  "v_exp_f32 v109, v109\nv_exp_f32 v110, v110\nv_exp_f32 v111, v111\nv_mfma_f32_32x32x16_bf16 v[64:79], v[152:155], v[128:131], 0\nv_exp_f32 v112, v112\n" \
  "v_exp_f32 v113, v113\nv_exp_f32 v114, v114\nv_exp_f32 v115, v115\nv_mfma_f32_32x32x8_bf16 v[64:79], v[156:157], v[144:145], v[64:79]\n" \
  "v_exp_f32 v116, v116\nv_exp_f32 v117, v117\nv_exp_f32 v118, v118\nv_exp_f32 v119, v119\n" \
  "v_smfmac_f32_32x32x32_bf16 v[32:47], v[158:161], v[96:103], v178\nv_exp_f32 v120, v120\nv_exp_f32 v121, v121\nv_exp_f32 v122, v122\n" \
  "v_exp_f32 v123, v123\nv_smfmac_f32_32x32x32_bf16 v[32:47], v[162:165], v[104:111], v178\nv_exp_f32 v124, v124\nv_exp_f32 v125, v125\n" \
  "v_exp_f32 v126, v126\nv_exp_f32 v127, v127\nv_mfma_f32_32x32x16_bf16 v[80:95], v[152:155], v[132:135], 0\nv_exp_f32 v64, v64\nv_exp_f32 v65, v65\n" \
  "v_exp_f32 v66, v66\nv_exp_f32 v67, v67\nv_mfma_f32_32x32x8_bf16 v[80:95], v[156:157], v[146:147], v[80:95]\nv_exp_f32 v68, v68\nv_exp_f32 v69, v69\n" \
  "v_exp_f32 v70, v70\nv_exp_f32 v71, v71\nv_smfmac_f32_32x32x32_bf16 v[48:63], v[158:161], v[112:119], v178\nds_read_b128 v[158:161], v171 offset:0\n" \
  "v_exp_f32 v72, v72\nv_exp_f32 v73, v73\nv_exp_f32 v74, v74\nv_exp_f32 v75, v75\nv_smfmac_f32_32x32x32_bf16 v[48:63], v[162:165], v[120:127], v178\n" \
  "ds_read_b128 v[162:165], v172 offset:0\nv_exp_f32 v76, v76\nv_exp_f32 v77, v77\nv_exp_f32 v78, v78\nv_exp_f32 v79, v79\n" \
  "v_mfma_f32_32x32x16_bf16 v[96:111], v[152:155], v[136:139], 0\nv_exp_f32 v80, v80\nv_exp_f32 v81, v81\nv_exp_f32 v82, v82\nv_exp_f32 v83, v83\n" \
  "v_mfma_f32_32x32x8_bf16 v[96:111], v[156:157], v[148:149], v[96:111]\ns_waitcnt lgkmcnt(0)\nv_exp_f32 v84, v84\nv_exp_f32 v85, v85\n" \
  "v_exp_f32 v86, v86\nv_exp_f32 v87, v87\nv_smfmac_f32_32x32x32_bf16 v[0:15], v[158:161], v[64:71], v178\nv_exp_f32 v88, v88\nv_exp_f32 v89, v89\n" \
  "v_exp_f32 v90, v90\nv_exp_f32 v91, v91\nv_smfmac_f32_32x32x32_bf16 v[0:15], v[162:165], v[72:79], v178\ns_waitcnt vmcnt(0)\nv_exp_f32 v92, v92\n" \
  "v_exp_f32 v93, v93\nv_exp_f32 v94, v94\nv_exp_f32 v95, v95\nv_mfma_f32_32x32x16_bf16 v[112:127], v[152:155], v[140:143], 0\n" \
  "ds_read_b128 v[152:155], v166 offset:20608\nv_exp_f32 v96, v96\nv_exp_f32 v97, v97\nv_exp_f32 v98, v98\nv_exp_f32 v99, v99\n" \
  "v_mfma_f32_32x32x8_bf16 v[112:127], v[156:157], v[150:151], v[112:127]\nds_read_b64 v[156:157], v167 offset:20608\nv_exp_f32 v100, v100\n" \
  "v_exp_f32 v101, v101\nv_exp_f32 v102, v102\nv_exp_f32 v103, v103\nv_smfmac_f32_32x32x32_bf16 v[16:31], v[158:161], v[80:87], v178\n" \
  "v_exp_f32 v104, v104\nv_exp_f32 v105, v105\nv_exp_f32 v106, v106\nv_exp_f32 v107, v107\n" \
  "v_smfmac_f32_32x32x32_bf16 v[16:31], v[162:165], v[88:95], v178\ns_waitcnt lgkmcnt(0)\nv_exp_f32 v108, v108\nv_exp_f32 v109, v109\n" \
  "v_exp_f32 v110, v110\nv_exp_f32 v111, v111\nv_mfma_f32_32x32x16_bf16 v[64:79], v[152:155], v[128:131], 0\nv_exp_f32 v112, v112\nv_exp_f32 v113, v113\n" \
  "v_exp_f32 v114, v114\nv_exp_f32 v115, v115\nv_mfma_f32_32x32x8_bf16 v[64:79], v[156:157], v[144:145], v[64:79]\nv_exp_f32 v116, v116\n" \
  "v_exp_f32 v117, v117\nv_exp_f32 v118, v118\nv_exp_f32 v119, v119\nv_smfmac_f32_32x32x32_bf16 v[32:47], v[158:161], v[96:103], v178\n" \
  "v_exp_f32 v120, v120\nv_exp_f32 v121, v121\nv_exp_f32 v122, v122\nv_exp_f32 v123, v123\n" \
  "v_smfmac_f32_32x32x32_bf16 v[32:47], v[162:165], v[104:111], v178\nv_exp_f32 v124, v124\nv_exp_f32 v125, v125\nv_exp_f32 v126, v126\n" \
  "v_exp_f32 v127, v127\nv_mfma_f32_32x32x16_bf16 v[80:95], v[152:155], v[132:135], 0\nv_exp_f32 v64, v64\nv_exp_f32 v65, v65\nv_exp_f32 v66, v66\n" \
  "v_exp_f32 v67, v67\nv_mfma_f32_32x32x8_bf16 v[80:95], v[156:157], v[146:147], v[80:95]\nv_exp_f32 v68, v68\nv_exp_f32 v69, v69\nv_exp_f32 v70, v70\n" \
  "v_exp_f32 v71, v71\nv_smfmac_f32_32x32x32_bf16 v[48:63], v[158:161], v[112:119], v178\nds_read_b128 v[158:161], v169 offset:20608\n" \
  "v_exp_f32 v72, v72\nv_exp_f32 v73, v73\nv_exp_f32 v74, v74\nv_exp_f32 v75, v75\nv_smfmac_f32_32x32x32_bf16 v[48:63], v[162:165], v[120:127], v178\n" \
  "ds_read_b128 v[162:165], v170 offset:20608\nv_exp_f32 v76, v76\nv_exp_f32 v77, v77\nv_exp_f32 v78, v78\nv_exp_f32 v79, v79\n" \
  "v_mfma_f32_32x32x16_bf16 v[96:111], v[152:155], v[136:139], 0\nv_exp_f32 v80, v80\nv_exp_f32 v81, v81\nv_exp_f32 v82, v82\nv_exp_f32 v83, v83\n" \
  "v_mfma_f32_32x32x8_bf16 v[96:111], v[156:157], v[148:149], v[96:111]\ns_waitcnt lgkmcnt(0)\nv_exp_f32 v84, v84\nv_exp_f32 v85, v85\n" \
  "v_exp_f32 v86, v86\nv_exp_f32 v87, v87\nv_smfmac_f32_32x32x32_bf16 v[0:15], v[158:161], v[64:71], v178\nv_exp_f32 v88, v88\nv_exp_f32 v89, v89\n" \
  "v_exp_f32 v90, v90\nv_exp_f32 v91, v91\nv_smfmac_f32_32x32x32_bf16 v[0:15], v[162:165], v[72:79], v178\nv_exp_f32 v92, v92\nv_exp_f32 v93, v93\n" \
  "v_exp_f32 v94, v94\nv_exp_f32 v95, v95\nv_mfma_f32_32x32x16_bf16 v[112:127], v[152:155], v[140:143], 0\nds_read_b128 v[152:155], v166 offset:21120\n" \
  "v_exp_f32 v96, v96\nv_exp_f32 v97, v97\nv_exp_f32 v98, v98\nv_exp_f32 v99, v99\n" \
  "v_mfma_f32_32x32x8_bf16 v[112:127], v[156:157], v[150:151], v[112:127]\nds_read_b64 v[156:157], v168 offset:20608\nv_exp_f32 v100, v100\n" \
  "v_exp_f32 v101, v101\nv_exp_f32 v102, v102\nv_exp_f32 v103, v103\nv_smfmac_f32_32x32x32_bf16 v[16:31], v[158:161], v[80:87], v178\n" \
  "v_exp_f32 v104, v104\nv_exp_f32 v105, v105\nv_exp_f32 v106, v106\nv_exp_f32 v107, v107\n" \
  "v_smfmac_f32_32x32x32_bf16 v[16:31], v[162:165], v[88:95], v178\ns_waitcnt lgkmcnt(0)\nv_exp_f32 v108, v108\nv_exp_f32 v109, v109\n" \
  "v_exp_f32 v110, v110\nv_exp_f32 v111, v111\nv_mfma_f32_32x32x16_bf16 v[64:79], v[152:155], v[128:131], 0\nv_exp_f32 v112, v112\nv_exp_f32 v113, v113\n" \
  "v_exp_f32 v114, v114\nv_exp_f32 v115, v115\nv_mfma_f32_32x32x8_bf16 v[64:79], v[156:157], v[144:145], v[64:79]\nv_exp_f32 v116, v116\n" \
  "v_exp_f32 v117, v117\nv_exp_f32 v118, v118\nv_exp_f32 v119, v119\nv_smfmac_f32_32x32x32_bf16 v[32:47], v[158:161], v[96:103], v178\n" \
  "v_exp_f32 v120, v120\nv_exp_f32 v121, v121\nv_exp_f32 v122, v122\nv_exp_f32 v123, v123\n" \
  "v_smfmac_f32_32x32x32_bf16 v[32:47], v[162:165], v[104:111], v178\nv_exp_f32 v124, v124\nv_exp_f32 v125, v125\nv_exp_f32 v126, v126\n" \
  "v_exp_f32 v127, v127\nv_mfma_f32_32x32x16_bf16 v[80:95], v[152:155], v[132:135], 0\nv_exp_f32 v64, v64\nv_exp_f32 v65, v65\nv_exp_f32 v66, v66\n" \
  "v_exp_f32 v67, v67\nv_mfma_f32_32x32x8_bf16 v[80:95], v[156:157], v[146:147], v[80:95]\nv_exp_f32 v68, v68\nv_exp_f32 v69, v69\nv_exp_f32 v70, v70\n" \
  "v_exp_f32 v71, v71\nv_smfmac_f32_32x32x32_bf16 v[48:63], v[158:161], v[112:119], v178\nds_read_b128 v[158:161], v171 offset:20608\n" \
  "v_exp_f32 v72, v72\nv_exp_f32 v73, v73\nv_exp_f32 v74, v74\nv_exp_f32 v75, v75\nv_smfmac_f32_32x32x32_bf16 v[48:63], v[162:165], v[120:127], v178\n" \
  "ds_read_b128 v[162:165], v172 offset:20608\nv_exp_f32 v76, v76\nv_exp_f32 v77, v77\nv_exp_f32 v78, v78\nv_exp_f32 v79, v79\n" \
  "v_mfma_f32_32x32x16_bf16 v[96:111], v[152:155], v[136:139], 0\nv_exp_f32 v80, v80\nv_exp_f32 v81, v81\nv_exp_f32 v82, v82\nv_exp_f32 v83, v83\n" \
  "v_mfma_f32_32x32x8_bf16 v[96:111], v[156:157], v[148:149], v[96:111]\ns_waitcnt lgkmcnt(0)\nv_exp_f32 v84, v84\nv_exp_f32 v85, v85\n" \
  "v_exp_f32 v86, v86\nv_exp_f32 v87, v87\nv_smfmac_f32_32x32x32_bf16 v[0:15], v[158:161], v[64:71], v178\nv_exp_f32 v88, v88\nv_exp_f32 v89, v89\n" \
  "v_exp_f32 v90, v90\nv_exp_f32 v91, v91\nv_smfmac_f32_32x32x32_bf16 v[0:15], v[162:165], v[72:79], v178\nv_exp_f32 v92, v92\nv_exp_f32 v93, v93\n" \
  "v_exp_f32 v94, v94\nv_exp_f32 v95, v95\nv_mfma_f32_32x32x16_bf16 v[112:127], v[152:155], v[140:143], 0\nv_exp_f32 v96, v96\nv_exp_f32 v97, v97\n" \
  "v_exp_f32 v98, v98\nv_exp_f32 v99, v99\nv_mfma_f32_32x32x8_bf16 v[112:127], v[156:157], v[150:151], v[112:127]\nv_exp_f32 v100, v100\n" \
  "v_exp_f32 v101, v101\nv_exp_f32 v102, v102\nv_exp_f32 v103, v103\nv_smfmac_f32_32x32x32_bf16 v[16:31], v[158:161], v[80:87], v178\n" \
  "ds_write_b128 v177, v[0:3] offset:0\nv_exp_f32 v104, v104\nv_exp_f32 v105, v105\nv_exp_f32 v106, v106\nv_exp_f32 v107, v107\n" \
  "v_smfmac_f32_32x32x32_bf16 v[16:31], v[162:165], v[88:95], v178\nds_write_b128 v177, v[4:7] offset:1024\nds_write_b128 v177, v[8:11] offset:2048\n" \
  "v_exp_f32 v108, v108\nv_exp_f32 v109, v109\nv_exp_f32 v110, v110\nv_exp_f32 v111, v111\nv_exp_f32 v112, v112\nv_exp_f32 v113, v113\n" \
  "v_exp_f32 v114, v114\nv_exp_f32 v115, v115\nv_exp_f32 v116, v116\nv_exp_f32 v117, v117\nv_exp_f32 v118, v118\nv_exp_f32 v119, v119\n" \
  "v_smfmac_f32_32x32x32_bf16 v[32:47], v[158:161], v[96:103], v178\nds_write_b128 v177, v[16:19] offset:20608\n" \
  "ds_write_b128 v177, v[20:23] offset:21632\nv_exp_f32 v120, v120\nv_exp_f32 v121, v121\nv_exp_f32 v122, v122\nv_exp_f32 v123, v123\n" \
  "v_smfmac_f32_32x32x32_bf16 v[32:47], v[162:165], v[104:111], v178\nds_write_b128 v177, v[24:27] offset:22656\nv_mov_b32 v64, 16\n" \
  "ds_write_b32 %[me], v64\nv_exp_f32 v124, v124\nv_exp_f32 v125, v125\nv_exp_f32 v126, v126\nv_exp_f32 v127, v127\ns_nop 0\n" \
  "v_smfmac_f32_32x32x32_bf16 v[48:63], v[158:161], v[112:119], v178\nv_smfmac_f32_32x32x32_bf16 v[48:63], v[162:165], v[120:127], v178\n" \
  "" ASTAMP(13) "" \
  "1:\nds_read_b32 v64, %[pa]\ns_waitcnt lgkmcnt(0)\nv_cmp_gt_u32 vcc, 16, v64\ns_cbranch_vccz 2f\ns_sub_u32 %[n], %[n], 1\ns_cbranch_scc0 1b\n2:\n" \
  "ds_read_b128 v[64:67], v179 offset:0\nds_read_b128 v[68:71], v179 offset:1024\nds_read_b128 v[72:75], v179 offset:2048\n" \
  "ds_read_b128 v[76:79], v179 offset:20608\nds_read_b128 v[80:83], v179 offset:21632\nds_read_b128 v[84:87], v179 offset:22656\ns_waitcnt lgkmcnt(3)\n" \
  "v_add_f32 v72, v72, v40\nv_add_f32 v73, v73, v41\nv_add_f32 v74, v74, v42\nv_mov_b32 v96, v72\nv_mov_b32 v98, v72\nv_add_f32 v75, v75, v43\n" \
  "v_add_f32 v64, v64, v32\nv_permlane32_swap_b32 v98, v96\nv_add_f32 v65, v65, v33\nv_add_f32 v66, v66, v34\nv_rcp_f32 v98, v96\n" \
  "v_add_f32 v67, v67, v35\nv_add_f32 v68, v68, v36\nv_add_f32 v69, v69, v37\nv_add_f32 v70, v70, v38\nv_add_f32 v71, v71, v39\nv_mul_f32 v64, v64, v98\n" \
  "v_mul_f32 v65, v65, v98\nv_mul_f32 v66, v66, v98\nv_mul_f32 v67, v67, v98\nv_mul_f32 v68, v68, v98\nv_mul_f32 v69, v69, v98\nv_mul_f32 v70, v70, v98\n" \
  "v_mul_f32 v71, v71, v98\nv_mul_f32 v72, v72, v98\nv_mul_f32 v73, v73, v98\nv_mul_f32 v74, v74, v98\nv_mul_f32 v75, v75, v98\n" \
  "v_cvt_pk_bf16_f32 v100, v64, v65\nv_cvt_pk_bf16_f32 v101, v66, v67\nv_cvt_pk_bf16_f32 v102, v68, v69\nv_cvt_pk_bf16_f32 v103, v70, v71\n" \
  "v_cvt_pk_bf16_f32 v104, v72, v73\nv_cvt_pk_bf16_f32 v105, v74, v75\ns_waitcnt lgkmcnt(0)\nv_add_f32 v84, v84, v56\nv_add_f32 v85, v85, v57\n" \
  "v_add_f32 v86, v86, v58\nv_mov_b32 v97, v84\nv_mov_b32 v98, v84\nv_add_f32 v87, v87, v59\nv_add_f32 v76, v76, v48\nv_permlane32_swap_b32 v98, v97\n" \
  "v_add_f32 v77, v77, v49\nv_add_f32 v78, v78, v50\nv_rcp_f32 v98, v97\nv_add_f32 v79, v79, v51\nv_add_f32 v80, v80, v52\nv_add_f32 v81, v81, v53\n" \
  "v_add_f32 v82, v82, v54\nv_add_f32 v83, v83, v55\nv_mul_f32 v76, v76, v98\nv_mul_f32 v77, v77, v98\nv_mul_f32 v78, v78, v98\nv_mul_f32 v79, v79, v98\n" \
  "v_mul_f32 v80, v80, v98\nv_mul_f32 v81, v81, v98\nv_mul_f32 v82, v82, v98\nv_mul_f32 v83, v83, v98\nv_mul_f32 v84, v84, v98\nv_mul_f32 v85, v85, v98\n" \
  "v_mul_f32 v86, v86, v98\nv_mul_f32 v87, v87, v98\nv_cvt_pk_bf16_f32 v106, v76, v77\nv_cvt_pk_bf16_f32 v107, v78, v79\n" \
  "v_cvt_pk_bf16_f32 v108, v80, v81\nv_cvt_pk_bf16_f32 v109, v82, v83\nv_cvt_pk_bf16_f32 v110, v84, v85\nv_cvt_pk_bf16_f32 v111, v86, v87\n" \
  "v_min_f32 v96, v96, v97\nv_dot2_f32_bf16 v98, v100, 0, 0\nv_dot2_f32_bf16 v99, v101, 0, 0\nv_dot2_f32_bf16 v98, v102, 0, v98\n" \
  "v_dot2_f32_bf16 v99, v103, 0, v99\nv_dot2_f32_bf16 v98, v104, 0, v98\nv_dot2_f32_bf16 v99, v105, 0, v99\nv_dot2_f32_bf16 v98, v106, 0, v98\n" \
  "v_dot2_f32_bf16 v99, v107, 0, v99\nv_dot2_f32_bf16 v98, v108, 0, v98\nv_dot2_f32_bf16 v99, v109, 0, v99\nv_dot2_f32_bf16 v98, v110, 0, v98\n" \
  "v_dot2_f32_bf16 v99, v111, 0, v99\nv_cmp_lt_f32 %[ok], %[lo], v96\ns_nop 2\nv_cmp_o_f32 vcc, v98, v99\ns_nop 1\ns_and_b64 %[ok], %[ok], vcc\n" \
  "s_cmp_eq_u64 %[ok], -1\ns_cbranch_scc0 3f\nglobal_store_dwordx4 v180, v[100:103], %[ws] sc1\nv_add_u32 v180, 40960, v180\ns_mov_b32 exec_hi, 0\n" \
  "global_store_dwordx2 v181, v[104:105], %[ws] sc1\nv_add_u32 v181, 40960, v181\nglobal_store_dwordx2 v181, v[110:111], %[ws] sc1\n" \
  "s_mov_b32 exec_hi, -1\nglobal_store_dwordx4 v180, v[106:109], %[ws] sc1\ns_branch 3f\n7:\ns_sleep 1\nds_read_b32 v182, %[sy] offset:32\n" \
  "s_sub_u32 %[n], %[n], 1\ns_cbranch_scc1 8b\ns_waitcnt lgkmcnt(0)\nv_cmp_gt_u32 vcc, %[tg], v182\ns_cbranch_vccnz 7b\ns_branch 8b\n3:\n" \
  ""
#define XDMA_ASM \
  "s_add_u32 m0, %[m], 0\ns_nop 0\nglobal_load_lds_dwordx4 %[v], %[g0]\ns_add_u32 m0, %[m], 5120\nv_xor_b32 %[t], 64, %[v]\n" \
  "global_load_lds_dwordx4 %[t], %[g1]\ns_add_u32 m0, %[m], 10240\nv_xor_b32 %[t], 128, %[v]\nglobal_load_lds_dwordx4 %[t], %[g2]\n" \
  "s_add_u32 m0, %[m], 15360\nv_xor_b32 %[t], 192, %[v]\nglobal_load_lds_dwordx4 %[t], %[g3]\n" \
  ""
#define KVW_ASM \
  "ds_read_b64_tr_b16 v[0:1], %[wa] offset:0\nds_read_b64_tr_b16 v[2:3], %[wa] offset:64\nds_read_b64_tr_b16 v[4:5], %[wa] offset:512\n" \
  "ds_read_b64_tr_b16 v[6:7], %[wa] offset:576\nds_read_b64_tr_b16 v[8:9], %[wa] offset:1024\nds_read_b64_tr_b16 v[10:11], %[wa] offset:1088\n" \
  "ds_read_b64_tr_b16 v[12:13], %[wa] offset:1536\nds_read_b64_tr_b16 v[14:15], %[wa] offset:1600\nds_read_b64_tr_b16 v[16:17], %[wa] offset:2048\n" \
  "ds_read_b64_tr_b16 v[18:19], %[wa] offset:2112\nds_read_b64_tr_b16 v[20:21], %[wa] offset:2560\nds_read_b64_tr_b16 v[22:23], %[wa] offset:2624\n" \
  "ds_read_b64_tr_b16 v[24:25], %[wa] offset:3072\nds_read_b64_tr_b16 v[26:27], %[wa] offset:3136\nds_read_b64_tr_b16 v[28:29], %[wa] offset:3584\n" \
  "ds_read_b64_tr_b16 v[30:31], %[wa] offset:3648\nds_read_b64_tr_b16 v[32:33], %[wa] offset:4096\nds_read_b64_tr_b16 v[34:35], %[wa] offset:4160\n" \
  "ds_read_b64_tr_b16 v[36:37], %[wa] offset:4608\nds_read_b64_tr_b16 v[38:39], %[wa] offset:4672\nds_read_b64_tr_b16 v[40:41], %[wa] offset:5120\n" \
  "ds_read_b64_tr_b16 v[42:43], %[wa] offset:5184\nds_read_b64_tr_b16 v[44:45], %[wa] offset:5632\nds_read_b64_tr_b16 v[46:47], %[wa] offset:5696\n" \
  "ds_read_b64_tr_b16 v[48:49], %[wa] offset:6144\nds_read_b64_tr_b16 v[50:51], %[wa] offset:6208\nds_read_b64_tr_b16 v[52:53], %[wa] offset:6656\n" \
  "ds_read_b64_tr_b16 v[54:55], %[wa] offset:6720\nds_read_b64_tr_b16 v[56:57], %[wa] offset:7168\nds_read_b64_tr_b16 v[58:59], %[wa] offset:7232\n" \
  "ds_read_b64_tr_b16 v[60:61], %[wa] offset:7680\nds_read_b64_tr_b16 v[62:63], %[wa] offset:7744\nds_read_b64_tr_b16 v[64:65], %[wa] offset:8192\n" \
  "ds_read_b64_tr_b16 v[66:67], %[wa] offset:8256\nds_read_b64_tr_b16 v[68:69], %[wa] offset:8704\nds_read_b64_tr_b16 v[70:71], %[wa] offset:8768\n" \
  "ds_read_b64_tr_b16 v[72:73], %[wa] offset:9216\nds_read_b64_tr_b16 v[74:75], %[wa] offset:9280\nds_read_b64_tr_b16 v[76:77], %[wa] offset:9728\n" \
  "ds_read_b64_tr_b16 v[78:79], %[wa] offset:9792\n" \
  ""
#define KVPROJ_ASM \
  "s_waitcnt vmcnt(0)\nds_read_b128 v[208:211], %[x0] offset:0\nds_read_b128 v[212:215], %[x1] offset:0\nds_read_b128 v[216:219], %[x2] offset:0\n" \
  "ds_read_b128 v[220:223], %[x3] offset:0\nds_read_b128 v[224:227], %[x0] offset:256\nds_read_b128 v[228:231], %[x1] offset:256\n" \
  "ds_read_b128 v[232:235], %[x2] offset:256\nds_read_b128 v[236:239], %[x3] offset:256\nds_read_b128 v[240:243], %[x0] offset:512\n" \
  "ds_read_b128 v[244:247], %[x1] offset:512\ns_waitcnt lgkmcnt(9)\nv_mfma_f32_16x16x32_bf16 %[d], v[0:3], v[208:211], 0\n" \
  "ds_read_b128 v[208:211], %[x2] offset:512\ns_waitcnt lgkmcnt(9)\nv_mfma_f32_16x16x32_bf16 %[d], v[4:7], v[212:215], %[d]\n" \
  "ds_read_b128 v[212:215], %[x3] offset:512\ns_waitcnt lgkmcnt(9)\nv_mfma_f32_16x16x32_bf16 %[d], v[8:11], v[216:219], %[d]\n" \
  "ds_read_b128 v[216:219], %[x0] offset:768\ns_waitcnt lgkmcnt(9)\nv_mfma_f32_16x16x32_bf16 %[d], v[12:15], v[220:223], %[d]\n" \
  "ds_read_b128 v[220:223], %[x1] offset:768\ns_waitcnt lgkmcnt(9)\nv_mfma_f32_16x16x32_bf16 %[d], v[16:19], v[224:227], %[d]\n" \
  "ds_read_b128 v[224:227], %[x2] offset:768\ns_waitcnt lgkmcnt(9)\nv_mfma_f32_16x16x32_bf16 %[d], v[20:23], v[228:231], %[d]\n" \
  "ds_read_b128 v[228:231], %[x3] offset:768\ns_waitcnt lgkmcnt(9)\nv_mfma_f32_16x16x32_bf16 %[d], v[24:27], v[232:235], %[d]\n" \
  "ds_read_b128 v[232:235], %[x0] offset:1024\ns_waitcnt lgkmcnt(9)\nv_mfma_f32_16x16x32_bf16 %[d], v[28:31], v[236:239], %[d]\n" \
  "ds_read_b128 v[236:239], %[x1] offset:1024\ns_waitcnt lgkmcnt(9)\nv_mfma_f32_16x16x32_bf16 %[d], v[32:35], v[240:243], %[d]\n" \
  "ds_read_b128 v[240:243], %[x2] offset:1024\ns_waitcnt lgkmcnt(9)\nv_mfma_f32_16x16x32_bf16 %[d], v[36:39], v[244:247], %[d]\n" \
  "ds_read_b128 v[244:247], %[x3] offset:1024\ns_waitcnt lgkmcnt(9)\nv_mfma_f32_16x16x32_bf16 %[d], v[40:43], v[208:211], %[d]\ns_waitcnt lgkmcnt(8)\n" \
  "v_mfma_f32_16x16x32_bf16 %[d], v[44:47], v[212:215], %[d]\ns_waitcnt lgkmcnt(7)\nv_mfma_f32_16x16x32_bf16 %[d], v[48:51], v[216:219], %[d]\n" \
  "s_waitcnt lgkmcnt(6)\nv_mfma_f32_16x16x32_bf16 %[d], v[52:55], v[220:223], %[d]\ns_waitcnt lgkmcnt(5)\n" \
  "v_mfma_f32_16x16x32_bf16 %[d], v[56:59], v[224:227], %[d]\ns_waitcnt lgkmcnt(4)\nv_mfma_f32_16x16x32_bf16 %[d], v[60:63], v[228:231], %[d]\n" \
  "s_waitcnt lgkmcnt(3)\nv_mfma_f32_16x16x32_bf16 %[d], v[64:67], v[232:235], %[d]\ns_waitcnt lgkmcnt(2)\n" \
  "v_mfma_f32_16x16x32_bf16 %[d], v[68:71], v[236:239], %[d]\ns_waitcnt lgkmcnt(1)\nv_mfma_f32_16x16x32_bf16 %[d], v[72:75], v[240:243], %[d]\n" \
  "s_waitcnt lgkmcnt(0)\nv_mfma_f32_16x16x32_bf16 %[d], v[76:79], v[244:247], %[d]\ns_nop 7\n" \
  ""
#define QLOOP_ASM \
  "ds_read_b32 v198, %[mp] offset:48\ns_waitcnt lgkmcnt(0)\nv_cmp_gt_u32 vcc, 1, v198\ns_cbranch_vccnz 10f\n20:\n" \
  "ds_read_b64_tr_b16 v[208:209], %[wa] offset:0\nds_read_b64_tr_b16 v[210:211], %[wa] offset:160\nds_read_b128 v[212:215], %[xb] offset:0\n" \
  "ds_read_b128 v[216:219], %[xb] offset:40960\nds_read_b64_tr_b16 v[220:221], %[wa] offset:2560\nds_read_b64_tr_b16 v[222:223], %[wa] offset:2720\n" \
  "v_xor_b32 v197, 32, %[xb]\nds_read_b128 v[224:227], v197 offset:0\nds_read_b128 v[228:231], v197 offset:40960\n" \
  "ds_read_b64_tr_b16 v[232:233], %[wa] offset:5120\nds_read_b64_tr_b16 v[234:235], %[wa] offset:5280\nv_xor_b32 v196, 64, %[xb]\n" \
  "ds_read_b128 v[236:239], v196 offset:0\nds_read_b128 v[240:243], v196 offset:40960\ns_waitcnt lgkmcnt(8)\n" \
  "v_mfma_f32_32x32x16_bf16 v[64:79], v[208:211], v[212:215], 0\nds_read_b32 v198, %[mp] offset:48\nds_read_b64_tr_b16 v[244:245], %[wa] offset:7680\n" \
  "ds_read_b64_tr_b16 v[246:247], %[wa] offset:7840\nv_xor_b32 v197, 96, %[xb]\nv_lshlrev_b32 v173, 4, %[ln]\n" \
  "v_mfma_f32_32x32x16_bf16 v[80:95], v[208:211], v[216:219], 0\nds_read_b128 v[248:251], v197 offset:0\nds_read_b128 v[252:255], v197 offset:40960\n" \
  "v_mov_b32 v203, 4\nds_write_b32 %[me], v203\nv_cmp_gt_u32 s[28:29], 32, %[ln]\ns_waitcnt lgkmcnt(10)\n" \
  "v_mfma_f32_32x32x16_bf16 v[64:79], v[220:223], v[224:227], v[64:79]\ns_waitcnt lgkmcnt(5)\nv_cmp_gt_u32 vcc, 2, v198\ns_cbranch_vccnz 11f\n21:\n" \
  "ds_read_b64_tr_b16 v[208:209], %[wa] offset:10240\nds_read_b64_tr_b16 v[210:211], %[wa] offset:10400\nv_xor_b32 v196, 128, %[xb]\n" \
  "v_mfma_f32_32x32x16_bf16 v[80:95], v[220:223], v[228:231], v[80:95]\nds_read_b128 v[212:215], v196 offset:0\n" \
  "ds_read_b128 v[216:219], v196 offset:40960\nv_and_b32 v177, 31, %[ln]\nv_add_u32 v194, 32, %[ln]\n" \
  "v_mfma_f32_32x32x16_bf16 v[64:79], v[232:235], v[236:239], v[64:79]\nds_read_b64_tr_b16 v[220:221], %[wa] offset:12800\n" \
  "ds_read_b64_tr_b16 v[222:223], %[wa] offset:12960\nv_xor_b32 v197, 160, %[xb]\nv_and_b32 v195, 0x70, v194\n" \
  "v_mfma_f32_32x32x16_bf16 v[80:95], v[232:235], v[240:243], v[80:95]\nds_read_b128 v[224:227], v197 offset:0\n" \
  "ds_read_b128 v[228:231], v197 offset:40960\nv_lshlrev_b32 v194, 4, v194\nv_xad_u32 v175, v194, v195, %[c2560]\ns_waitcnt lgkmcnt(9)\n" \
  "v_mfma_f32_32x32x16_bf16 v[64:79], v[244:247], v[248:251], v[64:79]\nds_read_b64_tr_b16 v[232:233], %[wa] offset:15360\n" \
  "ds_read_b64_tr_b16 v[234:235], %[wa] offset:15520\nv_xor_b32 v196, 192, %[xb]\nv_add_u32 v194, 0x60, %[ln]\n" \
  "v_mfma_f32_32x32x16_bf16 v[80:95], v[244:247], v[252:255], v[80:95]\nds_read_b128 v[236:239], v196 offset:0\n" \
  "ds_read_b128 v[240:243], v196 offset:40960\nv_and_b32 v195, 0x70, v194\nv_lshlrev_b32 v194, 4, v194\ns_waitcnt lgkmcnt(8)\n" \
  "v_mfma_f32_32x32x16_bf16 v[64:79], v[208:211], v[212:215], v[64:79]\nds_read_b32 v198, %[mp] offset:48\n" \
  "ds_read_b64_tr_b16 v[244:245], %[wa] offset:17920\nds_read_b64_tr_b16 v[246:247], %[wa] offset:18080\nv_xor_b32 v197, 224, %[xb]\n" \
  "v_xad_u32 v176, v194, v195, %[c2560]\nv_mfma_f32_32x32x16_bf16 v[80:95], v[208:211], v[216:219], v[80:95]\nds_read_b128 v[248:251], v197 offset:0\n" \
  "ds_read_b128 v[252:255], v197 offset:40960\nv_mov_b32 v203, 5\nds_write_b32 %[me], v203\nv_and_b32 v195, 0x70, v177\ns_waitcnt lgkmcnt(10)\n" \
  "v_mfma_f32_32x32x16_bf16 v[64:79], v[220:223], v[224:227], v[64:79]\ns_waitcnt lgkmcnt(5)\nv_cmp_gt_u32 vcc, 3, v198\ns_cbranch_vccnz 12f\n22:\n" \
  "ds_read_b64_tr_b16 v[208:209], %[wa] offset:20480\nds_read_b64_tr_b16 v[210:211], %[wa] offset:20640\n" \
  "v_mfma_f32_32x32x16_bf16 v[80:95], v[220:223], v[228:231], v[80:95]\nds_read_b128 v[212:215], %[xb] offset:256\n" \
  "ds_read_b128 v[216:219], %[xb] offset:41216\nv_lshlrev_b32 v194, 4, v177\nv_xad_u32 v174, v194, v195, %[c2560]\n" \
  "v_mfma_f32_32x32x16_bf16 v[64:79], v[232:235], v[236:239], v[64:79]\nds_read_b64_tr_b16 v[220:221], %[wa] offset:23040\n" \
  "ds_read_b64_tr_b16 v[222:223], %[wa] offset:23200\nv_xor_b32 v197, 32, %[xb]\nv_add_u32 v195, 0x800, v194\n" \
  "v_mfma_f32_32x32x16_bf16 v[80:95], v[232:235], v[240:243], v[80:95]\nds_read_b128 v[224:227], v197 offset:256\n" \
  "ds_read_b128 v[228:231], v197 offset:41216\nv_cndmask_b32 v174, v174, v195, s[28:29]\nv_add_u32 %[qz], %[sqz], v194\ns_waitcnt lgkmcnt(9)\n" \
  "v_mfma_f32_32x32x16_bf16 v[64:79], v[244:247], v[248:251], v[64:79]\nds_read_b64_tr_b16 v[232:233], %[wa] offset:25600\n" \
  "ds_read_b64_tr_b16 v[234:235], %[wa] offset:25760\nv_xor_b32 v196, 64, %[xb]\nv_and_b32 v195, 32, %[ln]\n" \
  "v_mfma_f32_32x32x16_bf16 v[80:95], v[244:247], v[252:255], v[80:95]\nds_read_b128 v[236:239], v196 offset:256\n" \
  "ds_read_b128 v[240:243], v196 offset:41216\nv_lshl_add_u32 v166, v195, 4, v173\nv_add_u32 v166, %[lb], v166\ns_waitcnt lgkmcnt(8)\n" \
  "v_mfma_f32_32x32x16_bf16 v[64:79], v[208:211], v[212:215], v[64:79]\nds_read_b32 v198, %[mp] offset:48\n" \
  "ds_read_b64_tr_b16 v[244:245], %[wa] offset:28160\nds_read_b64_tr_b16 v[246:247], %[wa] offset:28320\nv_xor_b32 v197, 96, %[xb]\n" \
  "v_lshl_add_u32 v194, %[ln], 3, %[lb]\nv_mfma_f32_32x32x16_bf16 v[80:95], v[208:211], v[216:219], v[80:95]\nds_read_b128 v[248:251], v197 offset:256\n" \
  "ds_read_b128 v[252:255], v197 offset:41216\nv_mov_b32 v203, 6\nds_write_b32 %[me], v203\nv_mov_b32 v195, %[lbz]\ns_waitcnt lgkmcnt(10)\n" \
  "v_mfma_f32_32x32x16_bf16 v[64:79], v[220:223], v[224:227], v[64:79]\ns_waitcnt lgkmcnt(5)\nv_cmp_gt_u32 vcc, 4, v198\ns_cbranch_vccnz 13f\n23:\n" \
  "ds_read_b64_tr_b16 v[208:209], %[wa] offset:30720\nds_read_b64_tr_b16 v[210:211], %[wa] offset:30880\nv_xor_b32 v196, 128, %[xb]\n" \
  "v_mfma_f32_32x32x16_bf16 v[80:95], v[220:223], v[228:231], v[80:95]\nds_read_b128 v[212:215], v196 offset:256\n" \
  "ds_read_b128 v[216:219], v196 offset:41216\nv_add_u32 v167, 0x800, v194\nv_add_u32 v168, 0x900, v194\n" \
  "v_mfma_f32_32x32x16_bf16 v[64:79], v[232:235], v[236:239], v[64:79]\nds_read_b64_tr_b16 v[220:221], %[wa] offset:33280\n" \
  "ds_read_b64_tr_b16 v[222:223], %[wa] offset:33440\nv_xor_b32 v197, 160, %[xb]\nv_cndmask_b32 v167, v195, v167, s[28:29]\n" \
  "v_mfma_f32_32x32x16_bf16 v[80:95], v[232:235], v[240:243], v[80:95]\nds_read_b128 v[224:227], v197 offset:256\n" \
  "ds_read_b128 v[228:231], v197 offset:41216\nv_cndmask_b32 v168, v195, v168, s[28:29]\nv_and_b32 v194, 4, v177\ns_waitcnt lgkmcnt(9)\n" \
  "v_mfma_f32_32x32x16_bf16 v[64:79], v[244:247], v[248:251], v[64:79]\nds_read_b64_tr_b16 v[232:233], %[wa] offset:35840\n" \
  "ds_read_b64_tr_b16 v[234:235], %[wa] offset:36000\nv_xor_b32 v196, 192, %[xb]\nv_and_b32 v195, 8, v177\n" \
  "v_mfma_f32_32x32x16_bf16 v[80:95], v[244:247], v[252:255], v[80:95]\nds_read_b128 v[236:239], v196 offset:256\n" \
  "ds_read_b128 v[240:243], v196 offset:41216\nv_lshlrev_b32 v194, 1, v194\nv_lshrrev_b32 v195, 1, v195\ns_waitcnt lgkmcnt(8)\n" \
  "v_mfma_f32_32x32x16_bf16 v[64:79], v[208:211], v[212:215], v[64:79]\nds_read_b32 v198, %[mp] offset:48\n" \
  "ds_read_b64_tr_b16 v[244:245], %[wa] offset:38400\nds_read_b64_tr_b16 v[246:247], %[wa] offset:38560\nv_xor_b32 v197, 224, %[xb]\n" \
  "v_or_b32 v194, v194, v195\nv_mfma_f32_32x32x16_bf16 v[80:95], v[208:211], v[216:219], v[80:95]\nds_read_b128 v[248:251], v197 offset:256\n" \
  "ds_read_b128 v[252:255], v197 offset:41216\nv_mov_b32 v203, 7\nds_write_b32 %[me], v203\nv_and_or_b32 v194, v177, 3, v194\ns_waitcnt lgkmcnt(10)\n" \
  "v_mfma_f32_32x32x16_bf16 v[64:79], v[220:223], v[224:227], v[64:79]\ns_waitcnt lgkmcnt(5)\nv_cmp_gt_u32 vcc, 5, v198\ns_cbranch_vccnz 14f\n24:\n" \
  "ds_read_b64_tr_b16 v[208:209], %[wa] offset:40960\nds_read_b64_tr_b16 v[210:211], %[wa] offset:41120\n" \
  "v_mfma_f32_32x32x16_bf16 v[80:95], v[220:223], v[228:231], v[80:95]\nds_read_b128 v[212:215], %[xb] offset:512\n" \
  "ds_read_b128 v[216:219], %[xb] offset:41472\nv_cmp_gt_u32 s[36:37], 16, v177\nv_cmp_gt_u32 s[38:39], 20, v177\n" \
  "v_mfma_f32_32x32x16_bf16 v[64:79], v[232:235], v[236:239], v[64:79]\nds_read_b64_tr_b16 v[220:221], %[wa] offset:43520\n" \
  "ds_read_b64_tr_b16 v[222:223], %[wa] offset:43680\nv_xor_b32 v197, 32, %[xb]\nv_cmp_eq_u32 s[40:41], 20, v177\n" \
  "v_mfma_f32_32x32x16_bf16 v[80:95], v[232:235], v[240:243], v[80:95]\nds_read_b128 v[224:227], v197 offset:512\n" \
  "ds_read_b128 v[228:231], v197 offset:41472\nv_cndmask_b32 v194, v177, v194, s[36:37]\nv_bfe_u32 v195, v194, 1, 3\ns_waitcnt lgkmcnt(9)\n" \
  "v_mfma_f32_32x32x16_bf16 v[64:79], v[244:247], v[248:251], v[64:79]\nds_read_b64_tr_b16 v[232:233], %[wa] offset:46080\n" \
  "ds_read_b64_tr_b16 v[234:235], %[wa] offset:46240\nv_xor_b32 v196, 64, %[xb]\nv_lshrrev_b32 v172, 5, %[ln]\n" \
  "v_mfma_f32_32x32x16_bf16 v[80:95], v[244:247], v[252:255], v[80:95]\nds_read_b128 v[236:239], v196 offset:512\n" \
  "ds_read_b128 v[240:243], v196 offset:41472\nv_xor_b32 v195, v195, v172\nv_lshlrev_b32 v195, 4, v195\ns_waitcnt lgkmcnt(8)\n" \
  "v_mfma_f32_32x32x16_bf16 v[64:79], v[208:211], v[212:215], v[64:79]\nds_read_b32 v198, %[mp] offset:48\n" \
  "ds_read_b64_tr_b16 v[244:245], %[wa] offset:48640\nds_read_b64_tr_b16 v[246:247], %[wa] offset:48800\nv_xor_b32 v197, 96, %[xb]\n" \
  "v_lshl_add_u32 v194, v194, 7, v195\nv_mfma_f32_32x32x16_bf16 v[80:95], v[208:211], v[216:219], v[80:95]\nds_read_b128 v[248:251], v197 offset:512\n" \
  "ds_read_b128 v[252:255], v197 offset:41472\nv_mov_b32 v203, 8\nds_write_b32 %[me], v203\nv_mov_b32 v195, %[lbv]\ns_waitcnt lgkmcnt(10)\n" \
  "v_mfma_f32_32x32x16_bf16 v[64:79], v[220:223], v[224:227], v[64:79]\ns_waitcnt lgkmcnt(5)\nv_cmp_gt_u32 vcc, 6, v198\ns_cbranch_vccnz 15f\n25:\n" \
  "ds_read_b64_tr_b16 v[208:209], %[wa] offset:0\nds_read_b64_tr_b16 v[210:211], %[wa] offset:160\nv_xor_b32 v196, 128, %[xb]\n" \
  "v_mfma_f32_32x32x16_bf16 v[80:95], v[220:223], v[228:231], v[80:95]\nds_read_b128 v[212:215], v196 offset:512\n" \
  "ds_read_b128 v[216:219], v196 offset:41472\nv_add_u32 v169, v194, v195\nv_xad_u32 v170, v194, 32, v195\n" \
  "v_mfma_f32_32x32x16_bf16 v[64:79], v[232:235], v[236:239], v[64:79]\nds_read_b64_tr_b16 v[220:221], %[wa] offset:2560\n" \
  "ds_read_b64_tr_b16 v[222:223], %[wa] offset:2720\nv_xor_b32 v197, 160, %[xb]\nv_xad_u32 v171, v194, 64, v195\n" \
  "v_mfma_f32_32x32x16_bf16 v[80:95], v[232:235], v[240:243], v[80:95]\nds_read_b128 v[224:227], v197 offset:512\n" \
  "ds_read_b128 v[228:231], v197 offset:41472\nv_xor_b32 v194, 0x60, v194\nv_add_u32 v172, v194, v195\ns_waitcnt lgkmcnt(9)\n" \
  "v_mfma_f32_32x32x16_bf16 v[64:79], v[244:247], v[248:251], v[64:79]\nds_read_b64_tr_b16 v[232:233], %[wa] offset:5120\n" \
  "ds_read_b64_tr_b16 v[234:235], %[wa] offset:5280\nv_xor_b32 v196, 192, %[xb]\nv_cndmask_b32 v194, 16, 0, s[40:41]\n" \
  "v_mfma_f32_32x32x16_bf16 v[80:95], v[244:247], v[252:255], v[80:95]\nds_read_b128 v[236:239], v196 offset:512\n" \
  "ds_read_b128 v[240:243], v196 offset:41472\nv_add_u32 v194, %[lbo], v194\nv_cndmask_b32 v169, v194, v169, s[38:39]\ns_waitcnt lgkmcnt(8)\n" \
  "v_mfma_f32_32x32x16_bf16 v[64:79], v[208:211], v[212:215], v[64:79]\nds_read_b32 v198, %[mp] offset:48\n" \
  "ds_read_b64_tr_b16 v[244:245], %[wa] offset:7680\nds_read_b64_tr_b16 v[246:247], %[wa] offset:7840\nv_xor_b32 v197, 224, %[xb]\n" \
  "v_cndmask_b32 v170, v194, v170, s[38:39]\nv_mfma_f32_32x32x16_bf16 v[80:95], v[208:211], v[216:219], v[80:95]\n" \
  "ds_read_b128 v[248:251], v197 offset:512\nds_read_b128 v[252:255], v197 offset:41472\nv_mov_b32 v203, 9\nds_write_b32 %[me], v203\n" \
  "v_cndmask_b32 v171, v194, v171, s[38:39]\ns_waitcnt lgkmcnt(10)\nv_mfma_f32_32x32x16_bf16 v[64:79], v[220:223], v[224:227], v[64:79]\n" \
  "s_waitcnt lgkmcnt(5)\nv_cmp_gt_u32 vcc, 7, v198\ns_cbranch_vccnz 16f\n26:\nds_read_b64_tr_b16 v[208:209], %[wa] offset:10240\n" \
  "ds_read_b64_tr_b16 v[210:211], %[wa] offset:10400\nv_mfma_f32_32x32x16_bf16 v[80:95], v[220:223], v[228:231], v[80:95]\n" \
  "ds_read_b128 v[212:215], %[xb] offset:768\nds_read_b128 v[216:219], %[xb] offset:41728\nv_cndmask_b32 v172, v194, v172, s[38:39]\n" \
  "v_bfe_u32 v194, %[ln], 4, 1\nv_mfma_f32_32x32x16_bf16 v[64:79], v[232:235], v[236:239], v[64:79]\nds_read_b64_tr_b16 v[220:221], %[wa] offset:12800\n" \
  "ds_read_b64_tr_b16 v[222:223], %[wa] offset:12960\nv_xor_b32 v197, 32, %[xb]\nv_mul_u32_u24 v194, 0x5000, v194\n" \
  "v_mfma_f32_32x32x16_bf16 v[80:95], v[232:235], v[240:243], v[80:95]\nds_read_b128 v[224:227], v197 offset:768\n" \
  "ds_read_b128 v[228:231], v197 offset:41728\nv_and_b32 v195, 15, %[ln]\nv_lshl_add_u32 v181, v195, 3, v194\ns_waitcnt lgkmcnt(9)\n" \
  "v_mfma_f32_32x32x16_bf16 v[64:79], v[244:247], v[248:251], v[64:79]\nds_read_b64_tr_b16 v[232:233], %[wa] offset:15360\n" \
  "ds_read_b64_tr_b16 v[234:235], %[wa] offset:15520\nv_xor_b32 v196, 64, %[xb]\nv_lshl_add_u32 v180, v195, 4, v194\n" \
  "v_mfma_f32_32x32x16_bf16 v[80:95], v[244:247], v[252:255], v[80:95]\nds_read_b128 v[236:239], v196 offset:768\n" \
  "ds_read_b128 v[240:243], v196 offset:41728\nv_and_b32 v195, 32, %[ln]\nv_lshl_add_u32 v180, v195, 3, v180\ns_waitcnt lgkmcnt(8)\n" \
  "v_mfma_f32_32x32x16_bf16 v[64:79], v[208:211], v[212:215], v[64:79]\nds_read_b32 v198, %[mp] offset:48\n" \
  "ds_read_b64_tr_b16 v[244:245], %[wa] offset:17920\nds_read_b64_tr_b16 v[246:247], %[wa] offset:18080\nv_xor_b32 v197, 96, %[xb]\n" \
  "v_add_u32 v180, %[so8], v180\nv_mfma_f32_32x32x16_bf16 v[80:95], v[208:211], v[216:219], v[80:95]\nds_read_b128 v[248:251], v197 offset:768\n" \
  "ds_read_b128 v[252:255], v197 offset:41728\nv_mov_b32 v203, 10\nds_write_b32 %[me], v203\nv_add_u32 v181, %[so4], v181\ns_waitcnt lgkmcnt(10)\n" \
  "v_mfma_f32_32x32x16_bf16 v[64:79], v[220:223], v[224:227], v[64:79]\ns_waitcnt lgkmcnt(5)\nv_cmp_gt_u32 vcc, 8, v198\ns_cbranch_vccnz 17f\n27:\n" \
  "ds_read_b64_tr_b16 v[208:209], %[wa] offset:20480\nds_read_b64_tr_b16 v[210:211], %[wa] offset:20640\nv_xor_b32 v196, 128, %[xb]\n" \
  "v_mfma_f32_32x32x16_bf16 v[80:95], v[220:223], v[228:231], v[80:95]\nds_read_b128 v[212:215], v196 offset:768\n" \
  "ds_read_b128 v[216:219], v196 offset:41728\nv_bfe_u32 v194, %[ln], 3, 1\nv_mul_u32_u24 v194, 20608, v194\n" \
  "v_mfma_f32_32x32x16_bf16 v[64:79], v[232:235], v[236:239], v[64:79]\nds_read_b64_tr_b16 v[220:221], %[wa] offset:23040\n" \
  "ds_read_b64_tr_b16 v[222:223], %[wa] offset:23200\nv_xor_b32 v197, 160, %[xb]\nv_and_b32 v195, 7, %[ln]\n" \
  "v_mfma_f32_32x32x16_bf16 v[80:95], v[232:235], v[240:243], v[80:95]\nds_read_b128 v[224:227], v197 offset:768\n" \
  "ds_read_b128 v[228:231], v197 offset:41728\nv_lshl_add_u32 v194, v195, 2, v194\nv_add_u32 %[spa], %[lbo], v194\ns_waitcnt lgkmcnt(9)\n" \
  "v_mfma_f32_32x32x16_bf16 v[64:79], v[244:247], v[248:251], v[64:79]\nds_read_b64_tr_b16 v[232:233], %[wa] offset:25600\n" \
  "ds_read_b64_tr_b16 v[234:235], %[wa] offset:25760\nv_xor_b32 v196, 192, %[xb]\nv_add_u32 v177, %[lb], v173\n" \
  "v_mfma_f32_32x32x16_bf16 v[80:95], v[244:247], v[252:255], v[80:95]\nds_read_b128 v[236:239], v196 offset:768\n" \
  "ds_read_b128 v[240:243], v196 offset:41728\nv_add_u32 v179, %[sey], v173\nv_add_u32 %[qx], %[sqx], v173\ns_waitcnt lgkmcnt(8)\n" \
  "v_mfma_f32_32x32x16_bf16 v[64:79], v[208:211], v[212:215], v[64:79]\nds_read_b32 v198, %[mp] offset:48\n" \
  "ds_read_b64_tr_b16 v[244:245], %[wa] offset:28160\nds_read_b64_tr_b16 v[246:247], %[wa] offset:28320\nv_xor_b32 v197, 224, %[xb]\n" \
  "v_add_u32 %[qy], %[sqy], v173\nv_mfma_f32_32x32x16_bf16 v[80:95], v[208:211], v[216:219], v[80:95]\nds_read_b128 v[248:251], v197 offset:768\n" \
  "ds_read_b128 v[252:255], v197 offset:41728\nv_mov_b32 v203, 11\nds_write_b32 %[me], v203\ns_waitcnt lgkmcnt(10)\n" \
  "v_mfma_f32_32x32x16_bf16 v[64:79], v[220:223], v[224:227], v[64:79]\ns_waitcnt lgkmcnt(5)\nv_cmp_gt_u32 vcc, 9, v198\ns_cbranch_vccnz 18f\n28:\n" \
  "ds_read_b64_tr_b16 v[208:209], %[wa] offset:30720\nds_read_b64_tr_b16 v[210:211], %[wa] offset:30880\n" \
  "v_mfma_f32_32x32x16_bf16 v[80:95], v[220:223], v[228:231], v[80:95]\nds_read_b128 v[212:215], %[xb] offset:1024\n" \
  "ds_read_b128 v[216:219], %[xb] offset:41984\nv_mfma_f32_32x32x16_bf16 v[64:79], v[232:235], v[236:239], v[64:79]\n" \
  "ds_read_b64_tr_b16 v[220:221], %[wa] offset:33280\nds_read_b64_tr_b16 v[222:223], %[wa] offset:33440\nv_xor_b32 v197, 32, %[xb]\n" \
  "v_mfma_f32_32x32x16_bf16 v[80:95], v[232:235], v[240:243], v[80:95]\nds_read_b128 v[224:227], v197 offset:1024\n" \
  "ds_read_b128 v[228:231], v197 offset:41984\ns_waitcnt lgkmcnt(9)\nv_mfma_f32_32x32x16_bf16 v[64:79], v[244:247], v[248:251], v[64:79]\n" \
  "ds_read_b64_tr_b16 v[232:233], %[wa] offset:35840\nds_read_b64_tr_b16 v[234:235], %[wa] offset:36000\nv_xor_b32 v196, 64, %[xb]\n" \
  "v_mfma_f32_32x32x16_bf16 v[80:95], v[244:247], v[252:255], v[80:95]\nds_read_b128 v[236:239], v196 offset:1024\n" \
  "ds_read_b128 v[240:243], v196 offset:41984\ns_waitcnt lgkmcnt(8)\nv_mfma_f32_32x32x16_bf16 v[64:79], v[208:211], v[212:215], v[64:79]\n" \
  "ds_read_b32 v198, %[mp] offset:48\nds_read_b64_tr_b16 v[244:245], %[wa] offset:38400\nds_read_b64_tr_b16 v[246:247], %[wa] offset:38560\n" \
  "v_xor_b32 v197, 96, %[xb]\nv_mfma_f32_32x32x16_bf16 v[80:95], v[208:211], v[216:219], v[80:95]\nds_read_b128 v[248:251], v197 offset:1024\n" \
  "ds_read_b128 v[252:255], v197 offset:41984\ns_waitcnt lgkmcnt(9)\nv_mfma_f32_32x32x16_bf16 v[64:79], v[220:223], v[224:227], v[64:79]\n" \
  "s_waitcnt lgkmcnt(4)\nv_cmp_gt_u32 vcc, 10, v198\ns_cbranch_vccnz 19f\n29:\nds_read_b64_tr_b16 v[208:209], %[wa] offset:40960\n" \
  "ds_read_b64_tr_b16 v[210:211], %[wa] offset:41120\nv_xor_b32 v196, 128, %[xb]\nv_mfma_f32_32x32x16_bf16 v[80:95], v[220:223], v[228:231], v[80:95]\n" \
  "ds_read_b128 v[212:215], v196 offset:1024\nds_read_b128 v[216:219], v196 offset:41984\n" \
  "v_mfma_f32_32x32x16_bf16 v[64:79], v[232:235], v[236:239], v[64:79]\nds_read_b64_tr_b16 v[220:221], %[wa] offset:43520\n" \
  "ds_read_b64_tr_b16 v[222:223], %[wa] offset:43680\nv_xor_b32 v197, 160, %[xb]\nv_mfma_f32_32x32x16_bf16 v[80:95], v[232:235], v[240:243], v[80:95]\n" \
  "ds_read_b128 v[224:227], v197 offset:1024\nds_read_b128 v[228:231], v197 offset:41984\nds_read_b128 v[198:201], %[mp]\n" \
  "ds_read_b32 v202, %[mp] offset:32\ns_waitcnt lgkmcnt(10)\nv_mfma_f32_32x32x16_bf16 v[64:79], v[244:247], v[248:251], v[64:79]\n" \
  "ds_read_b64_tr_b16 v[232:233], %[wa] offset:46080\nds_read_b64_tr_b16 v[234:235], %[wa] offset:46240\nv_xor_b32 v196, 192, %[xb]\n" \
  "v_mfma_f32_32x32x16_bf16 v[80:95], v[244:247], v[252:255], v[80:95]\nds_read_b128 v[236:239], v196 offset:1024\n" \
  "ds_read_b128 v[240:243], v196 offset:41984\ns_waitcnt lgkmcnt(10)\nv_mfma_f32_32x32x16_bf16 v[64:79], v[208:211], v[212:215], v[64:79]\n" \
  "ds_read_b64_tr_b16 v[244:245], %[wa] offset:48640\nds_read_b64_tr_b16 v[246:247], %[wa] offset:48800\nv_xor_b32 v197, 224, %[xb]\n" \
  "v_mfma_f32_32x32x16_bf16 v[80:95], v[208:211], v[216:219], v[80:95]\nds_read_b128 v[248:251], v197 offset:1024\n" \
  "ds_read_b128 v[252:255], v197 offset:41984\ns_waitcnt lgkmcnt(8)\nv_min3_u32 v198, v198, v199, v200\nv_min3_u32 v198, v198, v201, v202\n" \
  "v_cmp_gt_u32 vcc, 11, v198\ns_mov_b32 %[late], 1\ns_cbranch_vccnz 30f\ns_mov_b32 %[late], 0\ns_mov_b32 m0, %[lb]\ns_nop 0\n" \
  "global_load_lds_dwordx4 v173, %[kb] sc1\nglobal_load_lds_dwordx4 v173, %[kb] offset:1024 sc1\ns_add_u32 m0, %[lb], 2048\ns_nop 0\n" \
  "global_load_lds_dwordx4 v174, %[kb] sc1\ns_add_u32 m0, %[lb], 3072\ns_nop 0\nglobal_load_lds_dwordx4 v175, %[kb] sc1\ns_add_u32 m0, %[lb], 4096\n" \
  "s_nop 0\nglobal_load_lds_dwordx4 v176, %[kb] sc1\n30:\nv_mfma_f32_32x32x16_bf16 v[64:79], v[220:223], v[224:227], v[64:79]\n" \
  "v_mfma_f32_32x32x16_bf16 v[80:95], v[220:223], v[228:231], v[80:95]\ns_waitcnt lgkmcnt(4)\n" \
  "v_mfma_f32_32x32x16_bf16 v[64:79], v[232:235], v[236:239], v[64:79]\nv_mfma_f32_32x32x16_bf16 v[80:95], v[232:235], v[240:243], v[80:95]\n" \
  "s_waitcnt lgkmcnt(0)\nv_mfma_f32_32x32x16_bf16 v[64:79], v[244:247], v[248:251], v[64:79]\n" \
  "v_mfma_f32_32x32x16_bf16 v[80:95], v[244:247], v[252:255], v[80:95]\ns_branch 40f\n10:\nds_read_b32 v198, %[mp] offset:48\ns_sub_u32 %[n], %[n], 1\n" \
  "s_cbranch_scc1 20b\ns_waitcnt lgkmcnt(0)\nv_cmp_gt_u32 vcc, 1, v198\ns_cbranch_vccnz 10b\ns_branch 20b\n11:\nds_read_b32 v198, %[mp] offset:48\n" \
  "s_sub_u32 %[n], %[n], 1\ns_cbranch_scc1 21b\ns_waitcnt lgkmcnt(0)\nv_cmp_gt_u32 vcc, 2, v198\ns_cbranch_vccnz 11b\ns_branch 21b\n12:\n" \
  "ds_read_b32 v198, %[mp] offset:48\ns_sub_u32 %[n], %[n], 1\ns_cbranch_scc1 22b\ns_waitcnt lgkmcnt(0)\nv_cmp_gt_u32 vcc, 3, v198\ns_cbranch_vccnz 12b\n" \
  "s_branch 22b\n13:\nds_read_b32 v198, %[mp] offset:48\ns_sub_u32 %[n], %[n], 1\ns_cbranch_scc1 23b\ns_waitcnt lgkmcnt(0)\nv_cmp_gt_u32 vcc, 4, v198\n" \
  "s_cbranch_vccnz 13b\ns_branch 23b\n14:\nds_read_b32 v198, %[mp] offset:48\ns_sub_u32 %[n], %[n], 1\ns_cbranch_scc1 24b\ns_waitcnt lgkmcnt(0)\n" \
  "v_cmp_gt_u32 vcc, 5, v198\ns_cbranch_vccnz 14b\ns_branch 24b\n15:\nds_read_b32 v198, %[mp] offset:48\ns_sub_u32 %[n], %[n], 1\ns_cbranch_scc1 25b\n" \
  "s_waitcnt lgkmcnt(0)\nv_cmp_gt_u32 vcc, 6, v198\ns_cbranch_vccnz 15b\ns_branch 25b\n16:\nds_read_b32 v198, %[mp] offset:48\ns_sub_u32 %[n], %[n], 1\n" \
  "s_cbranch_scc1 26b\ns_waitcnt lgkmcnt(0)\nv_cmp_gt_u32 vcc, 7, v198\ns_cbranch_vccnz 16b\ns_branch 26b\n17:\nds_read_b32 v198, %[mp] offset:48\n" \
  "s_sub_u32 %[n], %[n], 1\ns_cbranch_scc1 27b\ns_waitcnt lgkmcnt(0)\nv_cmp_gt_u32 vcc, 8, v198\ns_cbranch_vccnz 17b\ns_branch 27b\n18:\n" \
  "ds_read_b32 v198, %[mp] offset:48\ns_sub_u32 %[n], %[n], 1\ns_cbranch_scc1 28b\ns_waitcnt lgkmcnt(0)\nv_cmp_gt_u32 vcc, 9, v198\ns_cbranch_vccnz 18b\n" \
  "s_branch 28b\n19:\nds_read_b32 v198, %[mp] offset:48\ns_sub_u32 %[n], %[n], 1\ns_cbranch_scc1 29b\ns_waitcnt lgkmcnt(0)\nv_cmp_gt_u32 vcc, 10, v198\n" \
  "s_cbranch_vccnz 19b\ns_branch 29b\n40:\nv_and_b32 v194, 4, %[ln]\nv_mov_b32 v195, 0x3f803f80\nv_cmp_eq_u32 vcc, 0, v194\nv_mov_b32 v158, 0\n" \
  "v_mov_b32 v159, 0\nv_mov_b32 v160, 0\nv_mov_b32 v161, 0\nv_cndmask_b32 v195, 0, v195, vcc\nv_mov_b32 v162, 0\nv_mov_b32 v163, 0\nv_mov_b32 v164, 0\n" \
  "v_mov_b32 v165, 0\nv_cvt_pk_bf16_f32 v136, v64, v65\nv_cvt_pk_bf16_f32 v137, v66, v67\nv_cvt_pk_bf16_f32 v138, v68, v69\n" \
  "v_cvt_pk_bf16_f32 v139, v70, v71\nv_cvt_pk_bf16_f32 v148, v72, v73\nv_cvt_pk_bf16_f32 v149, v74, v75\nds_write_b128 %[qx], v[136:139]\n" \
  "v_cvt_pk_bf16_f32 v140, v80, v81\nv_cvt_pk_bf16_f32 v141, v82, v83\nv_cvt_pk_bf16_f32 v142, v84, v85\nv_cvt_pk_bf16_f32 v143, v86, v87\n" \
  "v_cvt_pk_bf16_f32 v150, v88, v89\nv_cvt_pk_bf16_f32 v151, v90, v91\nds_write_b128 %[qx], v[140:143] offset:1024\ns_mov_b32 exec_hi, 0\n" \
  "ds_write_b128 %[qx], v[148:151] offset:2048\ns_mov_b32 exec_hi, -1\nv_mov_b32 v194, 14\nds_write_b32 %[me], v194\n" \
  "v_mfma_f32_32x32x16_bf16 v[0:15], v[158:161], v[158:161], 0\nv_mfma_f32_32x32x16_bf16 v[16:31], v[158:161], v[158:161], 0\n" \
  "v_mfma_f32_32x32x16_bf16 v[32:47], v[158:161], v[158:161], 0\nv_mfma_f32_32x32x16_bf16 v[48:63], v[158:161], v[158:161], 0\n" \
  "v_mfma_f32_32x32x16_bf16 v[112:127], v[158:161], v[158:161], 0\n50:\nds_read_b128 v[198:201], %[mp]\ns_waitcnt lgkmcnt(0)\n" \
  "v_min3_u32 v198, v198, v199, v200\nv_min_u32 v198, v198, v201\nv_cmp_gt_u32 vcc, 14, v198\ns_cbranch_vccz 51f\ns_sub_u32 %[n], %[n], 1\n" \
  "s_cbranch_scc0 50b\n51:\nds_read_b128 v[128:131], %[qy]\nds_read_b128 v[132:135], %[qy] offset:1024\nds_read_b128 v[144:147], %[qz]\n" \
  "ds_write_b32 %[spa], v195\nv_mov_b32 v194, 15\nds_write_b32 %[me], v194\n" \
  ""
#define LOADER_ASM \
  "10:\nds_read_b128 v[208:211], %[mp]\ns_waitcnt lgkmcnt(0)\nv_min3_u32 v208, v208, v209, v210\nv_min_u32 v208, v208, v211\nv_cmp_gt_u32 vcc, 1, v208\n" \
  "s_cbranch_vccz 20f\ns_sub_u32 %[n], %[n], 1\ns_cbranch_scc0 10b\n20:\nglobal_load_dword v222, %[w0], s[22:23]\n" \
  "global_load_dword v223, %[w1], s[22:23]\ns_add_u32 m0, %[lb], 0\ns_nop 0\nglobal_load_lds_dwordx4 %[v0], s[22:23]\ns_add_u32 m0, %[lb], 1024\n" \
  "s_nop 0\nglobal_load_lds_dwordx4 %[v1], s[22:23]\ns_add_u32 m0, %[lb], 2048\ns_nop 0\nglobal_load_lds_dwordx4 %[v2], s[22:23]\n" \
  "s_add_u32 m0, %[lb], 3072\ns_nop 0\nglobal_load_lds_dwordx4 %[v3], s[22:23]\ns_add_u32 m0, %[lb], 4096\ns_nop 0\n" \
  "global_load_lds_dwordx4 %[v4], s[22:23]\ns_add_u32 m0, %[lb], 5120\ns_add_u32 s22, s22, 40960\ns_addc_u32 s23, s23, 0\n" \
  "global_load_lds_dwordx4 %[v0], s[22:23]\ns_add_u32 m0, %[lb], 6144\ns_nop 0\nglobal_load_lds_dwordx4 %[v1], s[22:23]\ns_add_u32 m0, %[lb], 7168\n" \
  "s_nop 0\nglobal_load_lds_dwordx4 %[v2], s[22:23]\ns_add_u32 m0, %[lb], 8192\ns_nop 0\nglobal_load_lds_dwordx4 %[v3], s[22:23]\n" \
  "s_add_u32 m0, %[lb], 9216\ns_nop 0\nglobal_load_lds_dwordx4 %[v4], s[22:23]\ns_add_u32 m0, %[lb], 10240\ns_add_u32 s22, s22, 40960\n" \
  "s_addc_u32 s23, s23, 0\nglobal_load_lds_dwordx4 %[v0], s[22:23]\ns_add_u32 m0, %[lb], 11264\ns_nop 0\nglobal_load_lds_dwordx4 %[v1], s[22:23]\n" \
  "s_add_u32 m0, %[lb], 12288\ns_nop 0\nglobal_load_lds_dwordx4 %[v2], s[22:23]\ns_add_u32 m0, %[lb], 13312\ns_nop 0\n" \
  "global_load_lds_dwordx4 %[v3], s[22:23]\ns_add_u32 m0, %[lb], 14336\ns_nop 0\nglobal_load_lds_dwordx4 %[v4], s[22:23]\ns_add_u32 m0, %[lb], 15360\n" \
  "s_add_u32 s22, s22, 40960\ns_addc_u32 s23, s23, 0\nglobal_load_lds_dwordx4 %[v0], s[22:23]\ns_add_u32 m0, %[lb], 16384\ns_nop 0\n" \
  "global_load_lds_dwordx4 %[v1], s[22:23]\ns_add_u32 m0, %[lb], 17408\ns_nop 0\nglobal_load_lds_dwordx4 %[v2], s[22:23]\ns_add_u32 m0, %[lb], 18432\n" \
  "s_nop 0\nglobal_load_lds_dwordx4 %[v3], s[22:23]\ns_add_u32 m0, %[lb], 19456\ns_nop 0\nglobal_load_lds_dwordx4 %[v4], s[22:23]\n" \
  "s_add_u32 m0, %[lb], 20480\ns_add_u32 s22, s22, 40960\ns_addc_u32 s23, s23, 0\nglobal_load_lds_dwordx4 %[v0], s[22:23]\ns_add_u32 m0, %[lb], 21504\n" \
  "s_nop 0\nglobal_load_lds_dwordx4 %[v1], s[22:23]\ns_add_u32 m0, %[lb], 22528\ns_nop 0\nglobal_load_lds_dwordx4 %[v2], s[22:23]\n" \
  "s_add_u32 m0, %[lb], 23552\ns_nop 0\nglobal_load_lds_dwordx4 %[v3], s[22:23]\ns_add_u32 m0, %[lb], 24576\ns_nop 0\n" \
  "global_load_lds_dwordx4 %[v4], s[22:23]\ns_add_u32 m0, %[lb], 25600\ns_add_u32 s22, s22, 40960\ns_addc_u32 s23, s23, 0\n" \
  "global_load_lds_dwordx4 %[v0], s[22:23]\ns_add_u32 m0, %[lb], 26624\ns_nop 0\nglobal_load_lds_dwordx4 %[v1], s[22:23]\ns_add_u32 m0, %[lb], 27648\n" \
  "s_nop 0\nglobal_load_lds_dwordx4 %[v2], s[22:23]\ns_add_u32 m0, %[lb], 28672\ns_nop 0\nglobal_load_lds_dwordx4 %[v3], s[22:23]\n" \
  "s_add_u32 m0, %[lb], 29696\ns_nop 0\nglobal_load_lds_dwordx4 %[v4], s[22:23]\ns_add_u32 m0, %[lb], 30720\ns_add_u32 s22, s22, 40960\n" \
  "s_addc_u32 s23, s23, 0\nglobal_load_lds_dwordx4 %[v0], s[22:23]\ns_add_u32 m0, %[lb], 31744\ns_nop 0\nglobal_load_lds_dwordx4 %[v1], s[22:23]\n" \
  "s_add_u32 m0, %[lb], 32768\ns_nop 0\nglobal_load_lds_dwordx4 %[v2], s[22:23]\ns_add_u32 m0, %[lb], 33792\ns_nop 0\n" \
  "global_load_lds_dwordx4 %[v3], s[22:23]\ns_add_u32 m0, %[lb], 34816\ns_nop 0\nglobal_load_lds_dwordx4 %[v4], s[22:23]\ns_add_u32 m0, %[lb], 35840\n" \
  "s_add_u32 s22, s22, 40960\ns_addc_u32 s23, s23, 0\nglobal_load_lds_dwordx4 %[v0], s[22:23]\ns_add_u32 m0, %[lb], 36864\ns_nop 0\n" \
  "global_load_lds_dwordx4 %[v1], s[22:23]\ns_add_u32 m0, %[lb], 37888\ns_nop 0\nglobal_load_lds_dwordx4 %[v2], s[22:23]\ns_add_u32 m0, %[lb], 38912\n" \
  "s_nop 0\nglobal_load_lds_dwordx4 %[v3], s[22:23]\ns_add_u32 m0, %[lb], 39936\ns_nop 0\nglobal_load_lds_dwordx4 %[v4], s[22:23]\ns_setprio 3\n" \
  "s_waitcnt vmcnt(30)\nv_mov_b32 v212, 1\nds_write_b32 %[mp], v212 offset:48\n11:\nds_read_b128 v[208:211], %[mp]\ns_waitcnt lgkmcnt(0)\n" \
  "v_min3_u32 v208, v208, v209, v210\nv_min_u32 v208, v208, v211\nv_cmp_gt_u32 vcc, 3, v208\ns_cbranch_vccz 21f\ns_sub_u32 %[n], %[n], 1\n" \
  "s_cbranch_scc0 11b\n21:\nds_read_b128 v[216:219], %[kvs]\ns_mov_b64 exec, %[kvm]\ns_waitcnt lgkmcnt(0)\n" \
  "global_store_dwordx4 %[kvd], v[216:219], %[ws] sc1\ns_mov_b64 exec, -1\ns_add_u32 m0, %[lb], 40960\ns_add_u32 s22, s22, 40960\n" \
  "s_addc_u32 s23, s23, 0\nglobal_load_lds_dwordx4 %[v0], s[22:23]\ns_add_u32 m0, %[lb], 41984\ns_nop 0\nglobal_load_lds_dwordx4 %[v1], s[22:23]\n" \
  "s_add_u32 m0, %[lb], 43008\ns_nop 0\nglobal_load_lds_dwordx4 %[v2], s[22:23]\ns_add_u32 m0, %[lb], 44032\ns_nop 0\n" \
  "global_load_lds_dwordx4 %[v3], s[22:23]\ns_add_u32 m0, %[lb], 45056\ns_nop 0\nglobal_load_lds_dwordx4 %[v4], s[22:23]\ns_add_u32 m0, %[lb], 46080\n" \
  "s_add_u32 s22, s22, 40960\ns_addc_u32 s23, s23, 0\nglobal_load_lds_dwordx4 %[v0], s[22:23]\ns_add_u32 m0, %[lb], 47104\ns_nop 0\n" \
  "global_load_lds_dwordx4 %[v1], s[22:23]\ns_add_u32 m0, %[lb], 48128\ns_nop 0\nglobal_load_lds_dwordx4 %[v2], s[22:23]\ns_add_u32 m0, %[lb], 49152\n" \
  "s_nop 0\nglobal_load_lds_dwordx4 %[v3], s[22:23]\ns_add_u32 m0, %[lb], 50176\ns_nop 0\nglobal_load_lds_dwordx4 %[v4], s[22:23]\ns_waitcnt vmcnt(31)\n" \
  "v_mov_b32 v212, 2\nds_write_b32 %[mp], v212 offset:48\ns_waitcnt vmcnt(21)\nv_mov_b32 v212, 3\nds_write_b32 %[mp], v212 offset:48\n12:\n" \
  "ds_read_b128 v[208:211], %[mp]\ns_waitcnt lgkmcnt(0)\nv_min3_u32 v208, v208, v209, v210\nv_min_u32 v208, v208, v211\nv_cmp_gt_u32 vcc, 4, v208\n" \
  "s_cbranch_vccz 22f\ns_sub_u32 %[n], %[n], 1\ns_cbranch_scc0 12b\n22:\ns_add_u32 m0, %[lb], 0\ns_add_u32 s22, s22, 40960\ns_addc_u32 s23, s23, 0\n" \
  "global_load_lds_dwordx4 %[v0], s[22:23]\ns_add_u32 m0, %[lb], 1024\ns_nop 0\nglobal_load_lds_dwordx4 %[v1], s[22:23]\ns_add_u32 m0, %[lb], 2048\n" \
  "s_nop 0\nglobal_load_lds_dwordx4 %[v2], s[22:23]\ns_add_u32 m0, %[lb], 3072\ns_nop 0\nglobal_load_lds_dwordx4 %[v3], s[22:23]\n" \
  "s_add_u32 m0, %[lb], 4096\ns_nop 0\nglobal_load_lds_dwordx4 %[v4], s[22:23]\ns_add_u32 m0, %[lb], 5120\ns_add_u32 s22, s22, 40960\n" \
  "s_addc_u32 s23, s23, 0\nglobal_load_lds_dwordx4 %[v0], s[22:23]\ns_add_u32 m0, %[lb], 6144\ns_nop 0\nglobal_load_lds_dwordx4 %[v1], s[22:23]\n" \
  "s_add_u32 m0, %[lb], 7168\ns_nop 0\nglobal_load_lds_dwordx4 %[v2], s[22:23]\ns_add_u32 m0, %[lb], 8192\ns_nop 0\n" \
  "global_load_lds_dwordx4 %[v3], s[22:23]\ns_add_u32 m0, %[lb], 9216\ns_nop 0\nglobal_load_lds_dwordx4 %[v4], s[22:23]\ns_waitcnt vmcnt(21)\n" \
  "v_mov_b32 v212, 4\nds_write_b32 %[mp], v212 offset:48\ns_waitcnt vmcnt(20)\ns_mov_b64 exec, 0xff\nglobal_store_dwordx4 %[f1], %[tagv], %[ws] sc1\n" \
  "s_mov_b64 exec, -1\n13:\nds_read_b128 v[208:211], %[mp]\ns_waitcnt lgkmcnt(0)\nv_min3_u32 v208, v208, v209, v210\nv_min_u32 v208, v208, v211\n" \
  "v_cmp_gt_u32 vcc, 5, v208\ns_cbranch_vccz 23f\ns_sub_u32 %[n], %[n], 1\ns_cbranch_scc0 13b\n23:\ns_add_u32 m0, %[lb], 10240\n" \
  "s_add_u32 s22, s22, 40960\ns_addc_u32 s23, s23, 0\nglobal_load_lds_dwordx4 %[v0], s[22:23]\ns_add_u32 m0, %[lb], 11264\ns_nop 0\n" \
  "global_load_lds_dwordx4 %[v1], s[22:23]\ns_add_u32 m0, %[lb], 12288\ns_nop 0\nglobal_load_lds_dwordx4 %[v2], s[22:23]\ns_add_u32 m0, %[lb], 13312\n" \
  "s_nop 0\nglobal_load_lds_dwordx4 %[v3], s[22:23]\ns_add_u32 m0, %[lb], 14336\ns_nop 0\nglobal_load_lds_dwordx4 %[v4], s[22:23]\n" \
  "s_add_u32 m0, %[lb], 15360\ns_add_u32 s22, s22, 40960\ns_addc_u32 s23, s23, 0\nglobal_load_lds_dwordx4 %[v0], s[22:23]\ns_add_u32 m0, %[lb], 16384\n" \
  "s_nop 0\nglobal_load_lds_dwordx4 %[v1], s[22:23]\ns_add_u32 m0, %[lb], 17408\ns_nop 0\nglobal_load_lds_dwordx4 %[v2], s[22:23]\n" \
  "s_add_u32 m0, %[lb], 18432\ns_nop 0\nglobal_load_lds_dwordx4 %[v3], s[22:23]\ns_add_u32 m0, %[lb], 19456\ns_nop 0\n" \
  "global_load_lds_dwordx4 %[v4], s[22:23]\ns_waitcnt vmcnt(21)\nv_mov_b32 v212, 5\nds_write_b32 %[mp], v212 offset:48\n14:\n" \
  "ds_read_b128 v[208:211], %[mp]\ns_waitcnt lgkmcnt(0)\nv_min3_u32 v208, v208, v209, v210\nv_min_u32 v208, v208, v211\nv_cmp_gt_u32 vcc, 6, v208\n" \
  "s_cbranch_vccz 24f\ns_sub_u32 %[n], %[n], 1\ns_cbranch_scc0 14b\n24:\ns_add_u32 m0, %[lb], 20480\ns_add_u32 s22, s22, 40960\ns_addc_u32 s23, s23, 0\n" \
  "global_load_lds_dwordx4 %[v0], s[22:23]\ns_add_u32 m0, %[lb], 21504\ns_nop 0\nglobal_load_lds_dwordx4 %[v1], s[22:23]\ns_add_u32 m0, %[lb], 22528\n" \
  "s_nop 0\nglobal_load_lds_dwordx4 %[v2], s[22:23]\ns_add_u32 m0, %[lb], 23552\ns_nop 0\nglobal_load_lds_dwordx4 %[v3], s[22:23]\n" \
  "s_add_u32 m0, %[lb], 24576\ns_nop 0\nglobal_load_lds_dwordx4 %[v4], s[22:23]\ns_add_u32 m0, %[lb], 25600\ns_add_u32 s22, s22, 40960\n" \
  "s_addc_u32 s23, s23, 0\nglobal_load_lds_dwordx4 %[v0], s[22:23]\ns_add_u32 m0, %[lb], 26624\ns_nop 0\nglobal_load_lds_dwordx4 %[v1], s[22:23]\n" \
  "s_add_u32 m0, %[lb], 27648\ns_nop 0\nglobal_load_lds_dwordx4 %[v2], s[22:23]\ns_add_u32 m0, %[lb], 28672\ns_nop 0\n" \
  "global_load_lds_dwordx4 %[v3], s[22:23]\ns_add_u32 m0, %[lb], 29696\ns_nop 0\nglobal_load_lds_dwordx4 %[v4], s[22:23]\n" \
  "global_load_dwordx2 %[r0], %[q0], %[ws] sc1\ns_waitcnt vmcnt(22)\nv_mov_b32 v212, 6\nds_write_b32 %[mp], v212 offset:48\n15:\n" \
  "ds_read_b128 v[208:211], %[mp]\ns_waitcnt lgkmcnt(0)\nv_min3_u32 v208, v208, v209, v210\nv_min_u32 v208, v208, v211\nv_cmp_gt_u32 vcc, 7, v208\n" \
  "s_cbranch_vccz 25f\ns_sub_u32 %[n], %[n], 1\ns_cbranch_scc0 15b\n25:\ns_add_u32 m0, %[lb], 30720\ns_add_u32 s22, s22, 40960\ns_addc_u32 s23, s23, 0\n" \
  "global_load_lds_dwordx4 %[v0], s[22:23]\ns_add_u32 m0, %[lb], 31744\ns_nop 0\nglobal_load_lds_dwordx4 %[v1], s[22:23]\ns_add_u32 m0, %[lb], 32768\n" \
  "s_nop 0\nglobal_load_lds_dwordx4 %[v2], s[22:23]\ns_add_u32 m0, %[lb], 33792\ns_nop 0\nglobal_load_lds_dwordx4 %[v3], s[22:23]\n" \
  "s_add_u32 m0, %[lb], 34816\ns_nop 0\nglobal_load_lds_dwordx4 %[v4], s[22:23]\ns_add_u32 m0, %[lb], 35840\ns_add_u32 s22, s22, 40960\n" \
  "s_addc_u32 s23, s23, 0\nglobal_load_lds_dwordx4 %[v0], s[22:23]\ns_add_u32 m0, %[lb], 36864\ns_nop 0\nglobal_load_lds_dwordx4 %[v1], s[22:23]\n" \
  "s_add_u32 m0, %[lb], 37888\ns_nop 0\nglobal_load_lds_dwordx4 %[v2], s[22:23]\ns_add_u32 m0, %[lb], 38912\ns_nop 0\n" \
  "global_load_lds_dwordx4 %[v3], s[22:23]\ns_add_u32 m0, %[lb], 39936\ns_nop 0\nglobal_load_lds_dwordx4 %[v4], s[22:23]\ns_waitcnt vmcnt(21)\n" \
  "v_mov_b32 v212, 7\nds_write_b32 %[mp], v212 offset:48\n16:\nds_read_b128 v[208:211], %[mp]\ns_waitcnt lgkmcnt(0)\nv_min3_u32 v208, v208, v209, v210\n" \
  "v_min_u32 v208, v208, v211\nv_cmp_gt_u32 vcc, 8, v208\ns_cbranch_vccz 26f\ns_sub_u32 %[n], %[n], 1\ns_cbranch_scc0 16b\n26:\n" \
  "s_add_u32 m0, %[lb], 40960\ns_add_u32 s22, s22, 40960\ns_addc_u32 s23, s23, 0\nglobal_load_lds_dwordx4 %[v0], s[22:23]\ns_add_u32 m0, %[lb], 41984\n" \
  "s_nop 0\nglobal_load_lds_dwordx4 %[v1], s[22:23]\ns_add_u32 m0, %[lb], 43008\ns_nop 0\nglobal_load_lds_dwordx4 %[v2], s[22:23]\n" \
  "s_add_u32 m0, %[lb], 44032\ns_nop 0\nglobal_load_lds_dwordx4 %[v3], s[22:23]\ns_add_u32 m0, %[lb], 45056\ns_nop 0\n" \
  "global_load_lds_dwordx4 %[v4], s[22:23]\ns_add_u32 m0, %[lb], 46080\ns_add_u32 s22, s22, 40960\ns_addc_u32 s23, s23, 0\n" \
  "global_load_lds_dwordx4 %[v0], s[22:23]\ns_add_u32 m0, %[lb], 47104\ns_nop 0\nglobal_load_lds_dwordx4 %[v1], s[22:23]\ns_add_u32 m0, %[lb], 48128\n" \
  "s_nop 0\nglobal_load_lds_dwordx4 %[v2], s[22:23]\ns_add_u32 m0, %[lb], 49152\ns_nop 0\nglobal_load_lds_dwordx4 %[v3], s[22:23]\n" \
  "s_add_u32 m0, %[lb], 50176\ns_nop 0\nglobal_load_lds_dwordx4 %[v4], s[22:23]\nglobal_load_dwordx2 %[r1], %[q1], %[ws] sc1\n" \
  "global_load_dwordx2 %[r2], %[q2], %[ws] sc1\nglobal_load_dwordx2 %[r3], %[q3], %[ws] sc1\ns_waitcnt vmcnt(24)\nv_mov_b32 v212, 8\n" \
  "ds_write_b32 %[mp], v212 offset:48\ns_waitcnt vmcnt(23)\nv_cmp_ne_u64 vcc, %[tag], %[r0]\ns_cmp_lg_u32 vcc_lo, 0\ns_cbranch_scc1 31f\n" \
  "v_mov_b32 v212, 11\nds_write_b32 %[mp], v212 offset:32\n31:\nds_read_b64_tr_b16 v[224:225], %[xa0] offset:0\n" \
  "ds_read_b64_tr_b16 v[226:227], %[xc0] offset:0\nds_read_b64_tr_b16 v[228:229], %[xa0] offset:40960\n" \
  "ds_read_b64_tr_b16 v[230:231], %[xc0] offset:40960\nds_read_b64_tr_b16 v[232:233], %[xa1] offset:0\nds_read_b64_tr_b16 v[234:235], %[xc1] offset:0\n" \
  "s_waitcnt vmcnt(13)\nv_mov_b32 v212, 9\nds_write_b32 %[mp], v212 offset:48\ns_waitcnt lgkmcnt(4)\n" \
  "global_store_dwordx4 %[ud], v[224:227], %[ws] offset:0 sc1\ns_nop 1\nds_read_b64_tr_b16 v[236:237], %[xa1] offset:40960\n" \
  "ds_read_b64_tr_b16 v[238:239], %[xc1] offset:40960\ns_waitcnt lgkmcnt(4)\nglobal_store_dwordx4 %[ud], v[228:231], %[ws] offset:64 sc1\ns_nop 1\n" \
  "ds_read_b64_tr_b16 v[224:225], %[xa2] offset:0\nds_read_b64_tr_b16 v[226:227], %[xc2] offset:0\ns_waitcnt vmcnt(5)\nv_mov_b32 v212, 10\n" \
  "ds_write_b32 %[mp], v212 offset:48\nv_add_u32 %[ud], 65536, %[ud]\ns_waitcnt lgkmcnt(4)\nglobal_store_dwordx4 %[ud], v[232:235], %[ws] offset:0 sc1\n" \
  "s_nop 1\nds_read_b64_tr_b16 v[228:229], %[xa2] offset:40960\nds_read_b64_tr_b16 v[230:231], %[xc2] offset:40960\ns_waitcnt lgkmcnt(4)\n" \
  "global_store_dwordx4 %[ud], v[236:239], %[ws] offset:64 sc1\ns_nop 1\nds_read_b64_tr_b16 v[232:233], %[xa3] offset:0\n" \
  "ds_read_b64_tr_b16 v[234:235], %[xc3] offset:0\nv_add_u32 %[ud], 65536, %[ud]\ns_waitcnt lgkmcnt(4)\n" \
  "global_store_dwordx4 %[ud], v[224:227], %[ws] offset:0 sc1\ns_nop 1\nds_read_b64_tr_b16 v[236:237], %[xa3] offset:40960\n" \
  "ds_read_b64_tr_b16 v[238:239], %[xc3] offset:40960\ns_waitcnt lgkmcnt(4)\nglobal_store_dwordx4 %[ud], v[228:231], %[ws] offset:64 sc1\ns_nop 1\n" \
  "ds_read_b64_tr_b16 v[224:225], %[xa4] offset:0\nds_read_b64_tr_b16 v[226:227], %[xc4] offset:0\nv_add_u32 %[ud], 65536, %[ud]\ns_waitcnt lgkmcnt(4)\n" \
  "global_store_dwordx4 %[ud], v[232:235], %[ws] offset:0 sc1\ns_nop 1\nds_read_b64_tr_b16 v[228:229], %[xa4] offset:40960\n" \
  "ds_read_b64_tr_b16 v[230:231], %[xc4] offset:40960\ns_waitcnt lgkmcnt(4)\nglobal_store_dwordx4 %[ud], v[236:239], %[ws] offset:64 sc1\n" \
  "v_add_u32 %[ud], 65536, %[ud]\ns_waitcnt lgkmcnt(2)\nglobal_store_dwordx4 %[ud], v[224:227], %[ws] offset:0 sc1\ns_waitcnt lgkmcnt(0)\n" \
  "global_store_dwordx4 %[ud], v[228:231], %[ws] offset:64 sc1\ns_waitcnt vmcnt(10)\ns_mov_b32 s27, 0\n41:\ns_movk_i32 s26, 0x100\n" \
  "v_cmp_ne_u64 s[36:37], %[tag], %[r0]\nv_cmp_ne_u64 s[38:39], %[tag], %[r1]\nv_cmp_ne_u64 s[40:41], %[tag], %[r2]\n" \
  "v_cmp_ne_u64 s[42:43], %[tag], %[r3]\ns_cmp_lg_u32 s36, 0\ns_cselect_b32 s24, 1, 0\ns_cmp_lg_u32 s37, 0\ns_cselect_b32 s25, 2, 0\n" \
  "s_or_b32 s26, s26, s24\ns_or_b32 s26, s26, s25\ns_cmp_lg_u32 s38, 0\ns_cselect_b32 s24, 4, 0\ns_cmp_lg_u32 s39, 0\ns_cselect_b32 s25, 8, 0\n" \
  "s_or_b32 s26, s26, s24\ns_or_b32 s26, s26, s25\ns_cmp_lg_u32 s40, 0\ns_cselect_b32 s24, 16, 0\ns_cmp_lg_u32 s41, 0\ns_cselect_b32 s25, 32, 0\n" \
  "s_or_b32 s26, s26, s24\ns_or_b32 s26, s26, s25\ns_cmp_lg_u32 s42, 0\ns_cselect_b32 s24, 64, 0\ns_cmp_lg_u32 s43, 0\ns_cselect_b32 s25, 128, 0\n" \
  "s_or_b32 s26, s26, s24\ns_or_b32 s26, s26, s25\ns_ff1_i32_b32 s24, s26\ns_add_u32 s24, s24, 10\nv_mov_b32 v212, s24\n" \
  "ds_write_b32 %[mp], v212 offset:32\ns_cmp_eq_u32 s26, 0x100\ns_cbranch_scc1 42f\ns_mov_b64 exec, s[36:37]\n" \
  "global_load_dwordx2 %[r0], %[q0], %[ws] sc1\ns_mov_b64 exec, s[38:39]\nglobal_load_dwordx2 %[r1], %[q1], %[ws] sc1\ns_mov_b64 exec, s[40:41]\n" \
  "global_load_dwordx2 %[r2], %[q2], %[ws] sc1\ns_mov_b64 exec, s[42:43]\nglobal_load_dwordx2 %[r3], %[q3], %[ws] sc1\ns_mov_b64 exec, -1\n" \
  "s_waitcnt vmcnt(0)\ns_cmp_lg_u32 s27, 0\ns_cbranch_scc1 43f\ns_mov_b32 s27, 1\ns_mov_b64 exec, 0xff\nglobal_store_dwordx4 %[f2], %[tagv], %[ws] sc1\n" \
  "s_mov_b64 exec, -1\n43:\ns_sub_u32 %[n], %[n], 1\ns_cbranch_scc0 41b\n42:\ns_cmp_lg_u32 s27, 0\ns_cbranch_scc1 44f\ns_waitcnt vmcnt(0)\n" \
  "s_mov_b64 exec, 0xff\nglobal_store_dwordx4 %[f2], %[tagv], %[ws] sc1\ns_mov_b64 exec, -1\n44:\n" \
  ""
#define BUILD_ASM \
  "ds_read_u16 v176, %[i0] offset:%[kap]\nds_read_u16 v177, %[i1] offset:%[kap]\nds_read_u16 v178, %[i2] offset:%[kap]\n" \
  "ds_read_u16 v179, %[i3] offset:%[kap]\nds_read_u16 v180, %[i4] offset:%[kap]\nds_read_u16 v181, %[i5] offset:%[kap]\n" \
  "ds_read_u16 v182, %[i6] offset:%[kap]\nds_read_u16 v183, %[i7] offset:%[kap]\nds_read_u16 v184, %[i8] offset:%[kap]\n" \
  "ds_read_u16 v185, %[i9] offset:%[kap]\nds_read_u16 v186, %[i10] offset:%[kap]\nds_write_b128 %[cw], %[u] offset:17152\ns_waitcnt lgkmcnt(1)\n" \
  "v_lshl_or_b32 v160, v177, 16, v176\nv_lshl_or_b32 v168, v178, 16, v177\nv_lshl_or_b32 v161, v179, 16, v178\nv_lshl_or_b32 v169, v180, 16, v179\n" \
  "v_lshl_or_b32 v162, v181, 16, v180\nv_lshl_or_b32 v170, v182, 16, v181\nv_lshl_or_b32 v163, v183, 16, v182\nv_lshl_or_b32 v171, v184, 16, v183\n" \
  "v_lshl_or_b32 v164, v185, 16, v184\nv_lshl_or_b32 v172, v186, 16, v185\nds_write_b128 %[cw], v[160:163]\nds_write_b128 %[cw], v[168:171] offset:4160\n" \
  "ds_write2_b32 %[cx], v161, v162 offset1:1\nds_write2_b32 %[cx], v163, v164 offset0:2 offset1:3\nds_write2_b32 %[cy], v169, v170 offset1:1\n" \
  "ds_write2_b32 %[cy], v171, v172 offset0:2 offset1:3\n" \
  ""
#define MARKS_ASM \
  "v_mov_b32 v216, -1\nv_mov_b32 v217, -1\nv_mov_b32 v218, -1\nv_mov_b32 v219, -1\nglobal_store_dwordx4 %[mo], v[216:219], %[ws] sc1\n" \
  "global_store_dwordx4 %[mo], v[216:219], %[ws] offset:1024 sc1\ns_sleep 2\nv_add_u32 %[mo], 20480, %[mo]\n" \
  "global_store_dwordx4 %[mo], v[216:219], %[ws] sc1\nglobal_store_dwordx4 %[mo], v[216:219], %[ws] offset:1024 sc1\ns_sleep 2\n" \
  "v_add_u32 %[mo], 20480, %[mo]\nglobal_store_dwordx4 %[mo], v[216:219], %[ws] sc1\nglobal_store_dwordx4 %[mo], v[216:219], %[ws] offset:1024 sc1\n" \
  "s_sleep 2\nv_add_u32 %[mo], 20480, %[mo]\nglobal_store_dwordx4 %[mo], v[216:219], %[ws] sc1\n" \
  "global_store_dwordx4 %[mo], v[216:219], %[ws] offset:1024 sc1\ns_sleep 2\nglobal_store_dwordx4 %[mr], v[216:219], %[ws] sc1\n" \
  "v_add_u32 %[mr], 40960, %[mr]\nglobal_store_dwordx4 %[mr], v[216:219], %[ws] sc1\ns_sleep 2\n" \
  "global_store_dwordx4 %[ma0], v[216:219], %[ws] offset:0 sc1\nglobal_store_dwordx4 %[ma0], v[216:219], %[ws] offset:1024 sc1\ns_sleep 2\n" \
  "global_store_dwordx4 %[ma0], v[216:219], %[ws] offset:2048 sc1\nglobal_store_dwordx4 %[ma0], v[216:219], %[ws] offset:3072 sc1\ns_sleep 2\n" \
  "global_store_dwordx4 %[ma1], v[216:219], %[ws] offset:0 sc1\nglobal_store_dwordx4 %[ma1], v[216:219], %[ws] offset:1024 sc1\ns_sleep 2\n" \
  "global_store_dwordx4 %[ma1], v[216:219], %[ws] offset:2048 sc1\nglobal_store_dwordx4 %[ma1], v[216:219], %[ws] offset:3072 sc1\ns_sleep 2\n" \
  "global_store_dwordx4 %[ma2], v[216:219], %[ws] offset:0 sc1\nglobal_store_dwordx4 %[ma2], v[216:219], %[ws] offset:1024 sc1\ns_sleep 2\n" \
  "global_store_dwordx4 %[ma2], v[216:219], %[ws] offset:2048 sc1\nglobal_store_dwordx4 %[ma2], v[216:219], %[ws] offset:3072 sc1\n" \
  ""
#define OLOAD_ASM \
  "global_load_dwordx4 v[96:99], %[lo], %[ob0] offset:0 sc1\nglobal_load_dwordx4 v[100:103], %[lo], %[ob0] offset:1024 sc1\n" \
  "global_load_dwordx4 v[104:107], %[lo], %[ob0] offset:2048 sc1\nglobal_load_dwordx4 v[108:111], %[lo], %[ob0] offset:3072 sc1\n" \
  "global_load_dwordx4 v[112:115], %[lo], %[ob1] offset:0 sc1\nglobal_load_dwordx4 v[116:119], %[lo], %[ob1] offset:1024 sc1\n" \
  "global_load_dwordx4 v[120:123], %[lo], %[ob1] offset:2048 sc1\nglobal_load_dwordx4 v[124:127], %[lo], %[ob1] offset:3072 sc1\n" \
  "global_load_dwordx4 v[128:131], %[lo], %[ob2] offset:0 sc1\nglobal_load_dwordx4 v[132:135], %[lo], %[ob2] offset:1024 sc1\n" \
  "global_load_dwordx4 v[136:139], %[lo], %[ob2] offset:2048 sc1\nglobal_load_dwordx4 v[140:143], %[lo], %[ob2] offset:3072 sc1\n" \
  "global_load_dwordx4 v[144:147], %[lo], %[ob3] offset:0 sc1\nglobal_load_dwordx4 v[148:151], %[lo], %[ob3] offset:1024 sc1\n" \
  "global_load_dwordx4 v[152:155], %[lo], %[ob3] offset:2048 sc1\nglobal_load_dwordx4 v[156:159], %[lo], %[ob3] offset:3072 sc1\n" \
  "global_load_dwordx2 v[160:161], %[l2], %[ob4] offset:0 sc1\nglobal_load_dwordx2 v[162:163], %[l2], %[ob4] offset:128 sc1\n" \
  "global_load_dwordx2 v[164:165], %[l2], %[ob4] offset:1024 sc1\nglobal_load_dwordx2 v[166:167], %[l2], %[ob4] offset:1152 sc1\n" \
  "global_load_dwordx2 v[168:169], %[l2], %[ob4] offset:2048 sc1\nglobal_load_dwordx2 v[170:171], %[l2], %[ob4] offset:2176 sc1\n" \
  "global_load_dwordx2 v[172:173], %[l2], %[ob4] offset:3072 sc1\nglobal_load_dwordx2 v[174:175], %[l2], %[ob4] offset:3200 sc1\n" \
  ""
#define OPROJ_A_ASM \
  "ds_read_b64_tr_b16 v[208:209], %[a0] offset:0\nds_read_b64_tr_b16 v[210:211], %[a1] offset:0\nds_read_b64_tr_b16 v[212:213], %[a0] offset:32\n" \
  "ds_read_b64_tr_b16 v[214:215], %[a1] offset:32\nds_read_b64_tr_b16 v[216:217], %[a0] offset:64\nds_read_b64_tr_b16 v[218:219], %[a1] offset:64\n" \
  "ds_read_b64_tr_b16 v[220:221], %[a0] offset:96\nds_read_b64_tr_b16 v[222:223], %[a1] offset:96\nds_read_b64_tr_b16 v[224:225], %[a0] offset:128\n" \
  "ds_read_b64_tr_b16 v[226:227], %[a1] offset:128\nds_read_b64_tr_b16 v[228:229], %[a0] offset:5120\nds_read_b64_tr_b16 v[230:231], %[a1] offset:5120\n" \
  "ds_read_b64_tr_b16 v[232:233], %[a0] offset:5152\nds_read_b64_tr_b16 v[234:235], %[a1] offset:5152\ns_waitcnt lgkmcnt(12) vmcnt(23)\n" \
  "v_mfma_f32_16x16x32_bf16 %[d0], v[208:211], v[96:99], 0\nds_read_b64_tr_b16 v[236:237], %[a0] offset:5184\n" \
  "ds_read_b64_tr_b16 v[238:239], %[a1] offset:5184\nv_max3_u32 %[mx], v96, v98, 0\ns_waitcnt lgkmcnt(12)\n" \
  "v_mfma_f32_16x16x32_bf16 %[d1], v[212:215], v[96:99], 0\nds_read_b64_tr_b16 v[208:209], %[a0] offset:5216\n" \
  "ds_read_b64_tr_b16 v[210:211], %[a1] offset:5216\ns_waitcnt lgkmcnt(12)\nv_mfma_f32_16x16x32_bf16 %[d2], v[216:219], v[96:99], 0\n" \
  "ds_read_b64_tr_b16 v[212:213], %[a0] offset:5248\nds_read_b64_tr_b16 v[214:215], %[a1] offset:5248\ns_waitcnt lgkmcnt(12)\n" \
  "v_mfma_f32_16x16x32_bf16 %[d3], v[220:223], v[96:99], 0\nds_read_b64_tr_b16 v[216:217], %[a0] offset:10240\n" \
  "ds_read_b64_tr_b16 v[218:219], %[a1] offset:10240\ns_waitcnt lgkmcnt(12)\nv_mfma_f32_16x16x32_bf16 %[d4], v[224:227], v[96:99], 0\n" \
  "ds_read_b64_tr_b16 v[220:221], %[a0] offset:10272\nds_read_b64_tr_b16 v[222:223], %[a1] offset:10272\ns_waitcnt lgkmcnt(12) vmcnt(22)\n" \
  "v_mfma_f32_16x16x32_bf16 %[d0], v[228:231], v[100:103], %[d0]\nds_read_b64_tr_b16 v[224:225], %[a0] offset:10304\n" \
  "ds_read_b64_tr_b16 v[226:227], %[a1] offset:10304\nv_max3_u32 %[mx], v100, v102, %[mx]\ns_waitcnt lgkmcnt(12)\n" \
  "v_mfma_f32_16x16x32_bf16 %[d1], v[232:235], v[100:103], %[d1]\nds_read_b64_tr_b16 v[228:229], %[a0] offset:10336\n" \
  "ds_read_b64_tr_b16 v[230:231], %[a1] offset:10336\ns_waitcnt lgkmcnt(12)\nv_mfma_f32_16x16x32_bf16 %[d2], v[236:239], v[100:103], %[d2]\n" \
  "ds_read_b64_tr_b16 v[232:233], %[a0] offset:10368\nds_read_b64_tr_b16 v[234:235], %[a1] offset:10368\ns_waitcnt lgkmcnt(12)\n" \
  "v_mfma_f32_16x16x32_bf16 %[d3], v[208:211], v[100:103], %[d3]\nds_read_b64_tr_b16 v[236:237], %[a0] offset:15360\n" \
  "ds_read_b64_tr_b16 v[238:239], %[a1] offset:15360\ns_waitcnt lgkmcnt(12)\nv_mfma_f32_16x16x32_bf16 %[d4], v[212:215], v[100:103], %[d4]\n" \
  "ds_read_b64_tr_b16 v[208:209], %[a0] offset:15392\nds_read_b64_tr_b16 v[210:211], %[a1] offset:15392\ns_waitcnt lgkmcnt(12) vmcnt(21)\n" \
  "v_mfma_f32_16x16x32_bf16 %[d0], v[216:219], v[104:107], %[d0]\nds_read_b64_tr_b16 v[212:213], %[a0] offset:15424\n" \
  "ds_read_b64_tr_b16 v[214:215], %[a1] offset:15424\nv_max3_u32 %[mx], v104, v106, %[mx]\ns_waitcnt lgkmcnt(12)\n" \
  "v_mfma_f32_16x16x32_bf16 %[d1], v[220:223], v[104:107], %[d1]\nds_read_b64_tr_b16 v[216:217], %[a0] offset:15456\n" \
  "ds_read_b64_tr_b16 v[218:219], %[a1] offset:15456\ns_waitcnt lgkmcnt(12)\nv_mfma_f32_16x16x32_bf16 %[d2], v[224:227], v[104:107], %[d2]\n" \
  "ds_read_b64_tr_b16 v[220:221], %[a0] offset:15488\nds_read_b64_tr_b16 v[222:223], %[a1] offset:15488\ns_waitcnt lgkmcnt(12)\n" \
  "v_mfma_f32_16x16x32_bf16 %[d3], v[228:231], v[104:107], %[d3]\nds_read_b64_tr_b16 v[224:225], %[a0] offset:20480\n" \
  "ds_read_b64_tr_b16 v[226:227], %[a1] offset:20480\ns_waitcnt lgkmcnt(12)\nv_mfma_f32_16x16x32_bf16 %[d4], v[232:235], v[104:107], %[d4]\n" \
  "ds_read_b64_tr_b16 v[228:229], %[a0] offset:20512\nds_read_b64_tr_b16 v[230:231], %[a1] offset:20512\ns_waitcnt lgkmcnt(12) vmcnt(20)\n" \
  "v_mfma_f32_16x16x32_bf16 %[d0], v[236:239], v[108:111], %[d0]\nds_read_b64_tr_b16 v[232:233], %[a0] offset:20544\n" \
  "ds_read_b64_tr_b16 v[234:235], %[a1] offset:20544\nv_max3_u32 %[mx], v108, v110, %[mx]\ns_waitcnt lgkmcnt(12)\n" \
  "v_mfma_f32_16x16x32_bf16 %[d1], v[208:211], v[108:111], %[d1]\nds_read_b64_tr_b16 v[236:237], %[a0] offset:20576\n" \
  "ds_read_b64_tr_b16 v[238:239], %[a1] offset:20576\ns_waitcnt lgkmcnt(12)\nv_mfma_f32_16x16x32_bf16 %[d2], v[212:215], v[108:111], %[d2]\n" \
  "ds_read_b64_tr_b16 v[208:209], %[a0] offset:20608\nds_read_b64_tr_b16 v[210:211], %[a1] offset:20608\ns_waitcnt lgkmcnt(12)\n" \
  "v_mfma_f32_16x16x32_bf16 %[d3], v[216:219], v[108:111], %[d3]\nds_read_b64_tr_b16 v[212:213], %[a0] offset:25600\n" \
  "ds_read_b64_tr_b16 v[214:215], %[a1] offset:25600\ns_waitcnt lgkmcnt(12)\nv_mfma_f32_16x16x32_bf16 %[d4], v[220:223], v[108:111], %[d4]\n" \
  "ds_read_b64_tr_b16 v[216:217], %[a0] offset:25632\nds_read_b64_tr_b16 v[218:219], %[a1] offset:25632\ns_waitcnt lgkmcnt(12) vmcnt(19)\n" \
  "v_mfma_f32_16x16x32_bf16 %[d0], v[224:227], v[112:115], %[d0]\nds_read_b64_tr_b16 v[220:221], %[a0] offset:25664\n" \
  "ds_read_b64_tr_b16 v[222:223], %[a1] offset:25664\nv_max3_u32 %[mx], v112, v114, %[mx]\ns_waitcnt lgkmcnt(12)\n" \
  "v_mfma_f32_16x16x32_bf16 %[d1], v[228:231], v[112:115], %[d1]\nds_read_b64_tr_b16 v[224:225], %[a0] offset:25696\n" \
  "ds_read_b64_tr_b16 v[226:227], %[a1] offset:25696\ns_waitcnt lgkmcnt(12)\nv_mfma_f32_16x16x32_bf16 %[d2], v[232:235], v[112:115], %[d2]\n" \
  "ds_read_b64_tr_b16 v[228:229], %[a0] offset:25728\nds_read_b64_tr_b16 v[230:231], %[a1] offset:25728\ns_waitcnt lgkmcnt(12)\n" \
  "v_mfma_f32_16x16x32_bf16 %[d3], v[236:239], v[112:115], %[d3]\nds_read_b64_tr_b16 v[232:233], %[a0] offset:30720\n" \
  "ds_read_b64_tr_b16 v[234:235], %[a1] offset:30720\ns_waitcnt lgkmcnt(12)\nv_mfma_f32_16x16x32_bf16 %[d4], v[208:211], v[112:115], %[d4]\n" \
  "ds_read_b64_tr_b16 v[236:237], %[a0] offset:30752\nds_read_b64_tr_b16 v[238:239], %[a1] offset:30752\ns_waitcnt lgkmcnt(12) vmcnt(18)\n" \
  "v_mfma_f32_16x16x32_bf16 %[d0], v[212:215], v[116:119], %[d0]\nds_read_b64_tr_b16 v[208:209], %[a0] offset:30784\n" \
  "ds_read_b64_tr_b16 v[210:211], %[a1] offset:30784\nv_max3_u32 %[mx], v116, v118, %[mx]\ns_waitcnt lgkmcnt(12)\n" \
  "v_mfma_f32_16x16x32_bf16 %[d1], v[216:219], v[116:119], %[d1]\nds_read_b64_tr_b16 v[212:213], %[a0] offset:30816\n" \
  "ds_read_b64_tr_b16 v[214:215], %[a1] offset:30816\ns_waitcnt lgkmcnt(12)\nv_mfma_f32_16x16x32_bf16 %[d2], v[220:223], v[116:119], %[d2]\n" \
  "ds_read_b64_tr_b16 v[216:217], %[a0] offset:30848\nds_read_b64_tr_b16 v[218:219], %[a1] offset:30848\ns_waitcnt lgkmcnt(12)\n" \
  "v_mfma_f32_16x16x32_bf16 %[d3], v[224:227], v[116:119], %[d3]\nds_read_b64_tr_b16 v[220:221], %[a0] offset:35840\n" \
  "ds_read_b64_tr_b16 v[222:223], %[a1] offset:35840\ns_waitcnt lgkmcnt(12)\nv_mfma_f32_16x16x32_bf16 %[d4], v[228:231], v[116:119], %[d4]\n" \
  "ds_read_b64_tr_b16 v[224:225], %[a0] offset:35872\nds_read_b64_tr_b16 v[226:227], %[a1] offset:35872\ns_waitcnt lgkmcnt(12) vmcnt(17)\n" \
  "v_mfma_f32_16x16x32_bf16 %[d0], v[232:235], v[120:123], %[d0]\nds_read_b64_tr_b16 v[228:229], %[a0] offset:35904\n" \
  "ds_read_b64_tr_b16 v[230:231], %[a1] offset:35904\nv_max3_u32 %[mx], v120, v122, %[mx]\ns_waitcnt lgkmcnt(12)\n" \
  "v_mfma_f32_16x16x32_bf16 %[d1], v[236:239], v[120:123], %[d1]\nds_read_b64_tr_b16 v[232:233], %[a0] offset:35936\n" \
  "ds_read_b64_tr_b16 v[234:235], %[a1] offset:35936\ns_waitcnt lgkmcnt(12)\nv_mfma_f32_16x16x32_bf16 %[d2], v[208:211], v[120:123], %[d2]\n" \
  "ds_read_b64_tr_b16 v[236:237], %[a0] offset:35968\nds_read_b64_tr_b16 v[238:239], %[a1] offset:35968\ns_waitcnt lgkmcnt(12)\n" \
  "v_mfma_f32_16x16x32_bf16 %[d3], v[212:215], v[120:123], %[d3]\nds_read_b64_tr_b16 v[208:209], %[a0] offset:40960\n" \
  "ds_read_b64_tr_b16 v[210:211], %[a1] offset:40960\ns_waitcnt lgkmcnt(12)\nv_mfma_f32_16x16x32_bf16 %[d4], v[216:219], v[120:123], %[d4]\n" \
  "ds_read_b64_tr_b16 v[212:213], %[a0] offset:40992\nds_read_b64_tr_b16 v[214:215], %[a1] offset:40992\ns_waitcnt lgkmcnt(12) vmcnt(16)\n" \
  "v_mfma_f32_16x16x32_bf16 %[d0], v[220:223], v[124:127], %[d0]\nds_read_b64_tr_b16 v[216:217], %[a0] offset:41024\n" \
  "ds_read_b64_tr_b16 v[218:219], %[a1] offset:41024\nv_max3_u32 %[mx], v124, v126, %[mx]\ns_waitcnt lgkmcnt(12)\n" \
  "v_mfma_f32_16x16x32_bf16 %[d1], v[224:227], v[124:127], %[d1]\nds_read_b64_tr_b16 v[220:221], %[a0] offset:41056\n" \
  "ds_read_b64_tr_b16 v[222:223], %[a1] offset:41056\ns_waitcnt lgkmcnt(12)\nv_mfma_f32_16x16x32_bf16 %[d2], v[228:231], v[124:127], %[d2]\n" \
  "ds_read_b64_tr_b16 v[224:225], %[a0] offset:41088\nds_read_b64_tr_b16 v[226:227], %[a1] offset:41088\ns_waitcnt lgkmcnt(12)\n" \
  "v_mfma_f32_16x16x32_bf16 %[d3], v[232:235], v[124:127], %[d3]\nds_read_b64_tr_b16 v[228:229], %[a0] offset:46080\n" \
  "ds_read_b64_tr_b16 v[230:231], %[a1] offset:46080\ns_waitcnt lgkmcnt(12)\nv_mfma_f32_16x16x32_bf16 %[d4], v[236:239], v[124:127], %[d4]\n" \
  "ds_read_b64_tr_b16 v[232:233], %[a0] offset:46112\nds_read_b64_tr_b16 v[234:235], %[a1] offset:46112\ns_waitcnt lgkmcnt(12) vmcnt(15)\n" \
  "v_mfma_f32_16x16x32_bf16 %[d0], v[208:211], v[128:131], %[d0]\nds_read_b64_tr_b16 v[236:237], %[a0] offset:46144\n" \
  "ds_read_b64_tr_b16 v[238:239], %[a1] offset:46144\nv_max3_u32 %[mx], v128, v130, %[mx]\ns_waitcnt lgkmcnt(12)\n" \
  "v_mfma_f32_16x16x32_bf16 %[d1], v[212:215], v[128:131], %[d1]\nds_read_b64_tr_b16 v[208:209], %[a0] offset:46176\n" \
  "ds_read_b64_tr_b16 v[210:211], %[a1] offset:46176\ns_waitcnt lgkmcnt(12)\nv_mfma_f32_16x16x32_bf16 %[d2], v[216:219], v[128:131], %[d2]\n" \
  "ds_read_b64_tr_b16 v[212:213], %[a0] offset:46208\nds_read_b64_tr_b16 v[214:215], %[a1] offset:46208\ns_waitcnt lgkmcnt(12)\n" \
  "v_mfma_f32_16x16x32_bf16 %[d3], v[220:223], v[128:131], %[d3]\nds_read_b64_tr_b16 v[216:217], %[a0] offset:51200\n" \
  "ds_read_b64_tr_b16 v[218:219], %[a1] offset:51200\ns_waitcnt lgkmcnt(12)\nv_mfma_f32_16x16x32_bf16 %[d4], v[224:227], v[128:131], %[d4]\n" \
  "ds_read_b64_tr_b16 v[220:221], %[a0] offset:51232\nds_read_b64_tr_b16 v[222:223], %[a1] offset:51232\ns_waitcnt lgkmcnt(12) vmcnt(14)\n" \
  "v_mfma_f32_16x16x32_bf16 %[d0], v[228:231], v[132:135], %[d0]\nds_read_b64_tr_b16 v[224:225], %[a0] offset:51264\n" \
  "ds_read_b64_tr_b16 v[226:227], %[a1] offset:51264\nv_max3_u32 %[mx], v132, v134, %[mx]\ns_waitcnt lgkmcnt(12)\n" \
  "v_mfma_f32_16x16x32_bf16 %[d1], v[232:235], v[132:135], %[d1]\nds_read_b64_tr_b16 v[228:229], %[a0] offset:51296\n" \
  "ds_read_b64_tr_b16 v[230:231], %[a1] offset:51296\ns_waitcnt lgkmcnt(12)\nv_mfma_f32_16x16x32_bf16 %[d2], v[236:239], v[132:135], %[d2]\n" \
  "ds_read_b64_tr_b16 v[232:233], %[a0] offset:51328\nds_read_b64_tr_b16 v[234:235], %[a1] offset:51328\ns_waitcnt lgkmcnt(12)\n" \
  "v_mfma_f32_16x16x32_bf16 %[d3], v[208:211], v[132:135], %[d3]\nds_read_b64_tr_b16 v[236:237], %[a0] offset:56320\n" \
  "ds_read_b64_tr_b16 v[238:239], %[a1] offset:56320\ns_waitcnt lgkmcnt(12)\nv_mfma_f32_16x16x32_bf16 %[d4], v[212:215], v[132:135], %[d4]\n" \
  "ds_read_b64_tr_b16 v[208:209], %[a0] offset:56352\nds_read_b64_tr_b16 v[210:211], %[a1] offset:56352\ns_waitcnt lgkmcnt(12) vmcnt(13)\n" \
  "v_mfma_f32_16x16x32_bf16 %[d0], v[216:219], v[136:139], %[d0]\nds_read_b64_tr_b16 v[212:213], %[a0] offset:56384\n" \
  "ds_read_b64_tr_b16 v[214:215], %[a1] offset:56384\nv_max3_u32 %[mx], v136, v138, %[mx]\ns_waitcnt lgkmcnt(12)\n" \
  "v_mfma_f32_16x16x32_bf16 %[d1], v[220:223], v[136:139], %[d1]\nds_read_b64_tr_b16 v[216:217], %[a0] offset:56416\n" \
  "ds_read_b64_tr_b16 v[218:219], %[a1] offset:56416\ns_waitcnt lgkmcnt(12)\nv_mfma_f32_16x16x32_bf16 %[d2], v[224:227], v[136:139], %[d2]\n" \
  "ds_read_b64_tr_b16 v[220:221], %[a0] offset:56448\nds_read_b64_tr_b16 v[222:223], %[a1] offset:56448\ns_waitcnt lgkmcnt(12)\n" \
  "v_mfma_f32_16x16x32_bf16 %[d3], v[228:231], v[136:139], %[d3]\nds_read_b64_tr_b16 v[224:225], %[b0] offset:0\n" \
  "ds_read_b64_tr_b16 v[226:227], %[b1] offset:0\ns_waitcnt lgkmcnt(12)\nv_mfma_f32_16x16x32_bf16 %[d4], v[232:235], v[136:139], %[d4]\n" \
  "ds_read_b64_tr_b16 v[228:229], %[b0] offset:32\nds_read_b64_tr_b16 v[230:231], %[b1] offset:32\ns_waitcnt lgkmcnt(12) vmcnt(12)\n" \
  "v_mfma_f32_16x16x32_bf16 %[d0], v[236:239], v[140:143], %[d0]\nds_read_b64_tr_b16 v[232:233], %[b0] offset:64\n" \
  "ds_read_b64_tr_b16 v[234:235], %[b1] offset:64\nv_max3_u32 %[mx], v140, v142, %[mx]\ns_waitcnt lgkmcnt(12)\n" \
  "v_mfma_f32_16x16x32_bf16 %[d1], v[208:211], v[140:143], %[d1]\nds_read_b64_tr_b16 v[236:237], %[b0] offset:96\n" \
  "ds_read_b64_tr_b16 v[238:239], %[b1] offset:96\ns_waitcnt lgkmcnt(12)\nv_mfma_f32_16x16x32_bf16 %[d2], v[212:215], v[140:143], %[d2]\n" \
  "ds_read_b64_tr_b16 v[208:209], %[b0] offset:128\nds_read_b64_tr_b16 v[210:211], %[b1] offset:128\ns_waitcnt lgkmcnt(12)\n" \
  "v_mfma_f32_16x16x32_bf16 %[d3], v[216:219], v[140:143], %[d3]\nds_read_b64_tr_b16 v[212:213], %[b0] offset:5120\n" \
  "ds_read_b64_tr_b16 v[214:215], %[b1] offset:5120\ns_waitcnt lgkmcnt(12)\nv_mfma_f32_16x16x32_bf16 %[d4], v[220:223], v[140:143], %[d4]\n" \
  "ds_read_b64_tr_b16 v[216:217], %[b0] offset:5152\nds_read_b64_tr_b16 v[218:219], %[b1] offset:5152\ns_waitcnt lgkmcnt(12) vmcnt(11)\n" \
  "v_mfma_f32_16x16x32_bf16 %[d0], v[224:227], v[144:147], %[d0]\nds_read_b64_tr_b16 v[220:221], %[b0] offset:5184\n" \
  "ds_read_b64_tr_b16 v[222:223], %[b1] offset:5184\nv_max3_u32 %[mx], v144, v146, %[mx]\ns_waitcnt lgkmcnt(12)\n" \
  "v_mfma_f32_16x16x32_bf16 %[d1], v[228:231], v[144:147], %[d1]\nds_read_b64_tr_b16 v[224:225], %[b0] offset:5216\n" \
  "ds_read_b64_tr_b16 v[226:227], %[b1] offset:5216\ns_waitcnt lgkmcnt(12)\nv_mfma_f32_16x16x32_bf16 %[d2], v[232:235], v[144:147], %[d2]\n" \
  "ds_read_b64_tr_b16 v[228:229], %[b0] offset:5248\nds_read_b64_tr_b16 v[230:231], %[b1] offset:5248\ns_waitcnt lgkmcnt(12)\n" \
  "v_mfma_f32_16x16x32_bf16 %[d3], v[236:239], v[144:147], %[d3]\nds_read_b64_tr_b16 v[232:233], %[b0] offset:10240\n" \
  "ds_read_b64_tr_b16 v[234:235], %[b1] offset:10240\ns_waitcnt lgkmcnt(12)\nv_mfma_f32_16x16x32_bf16 %[d4], v[208:211], v[144:147], %[d4]\n" \
  "ds_read_b64_tr_b16 v[236:237], %[b0] offset:10272\nds_read_b64_tr_b16 v[238:239], %[b1] offset:10272\ns_waitcnt lgkmcnt(12) vmcnt(10)\n" \
  "v_mfma_f32_16x16x32_bf16 %[d0], v[212:215], v[148:151], %[d0]\nds_read_b64_tr_b16 v[208:209], %[b0] offset:10304\n" \
  "ds_read_b64_tr_b16 v[210:211], %[b1] offset:10304\nv_max3_u32 %[mx], v148, v150, %[mx]\ns_waitcnt lgkmcnt(12)\n" \
  "v_mfma_f32_16x16x32_bf16 %[d1], v[216:219], v[148:151], %[d1]\nds_read_b64_tr_b16 v[212:213], %[b0] offset:10336\n" \
  "ds_read_b64_tr_b16 v[214:215], %[b1] offset:10336\ns_waitcnt lgkmcnt(12)\nv_mfma_f32_16x16x32_bf16 %[d2], v[220:223], v[148:151], %[d2]\n" \
  "ds_read_b64_tr_b16 v[216:217], %[b0] offset:10368\nds_read_b64_tr_b16 v[218:219], %[b1] offset:10368\ns_waitcnt lgkmcnt(12)\n" \
  "v_mfma_f32_16x16x32_bf16 %[d3], v[224:227], v[148:151], %[d3]\nds_read_b64_tr_b16 v[220:221], %[b0] offset:15360\n" \
  "ds_read_b64_tr_b16 v[222:223], %[b1] offset:15360\ns_waitcnt lgkmcnt(12)\nv_mfma_f32_16x16x32_bf16 %[d4], v[228:231], v[148:151], %[d4]\n" \
  "ds_read_b64_tr_b16 v[224:225], %[b0] offset:15392\nds_read_b64_tr_b16 v[226:227], %[b1] offset:15392\ns_waitcnt lgkmcnt(12) vmcnt(9)\n" \
  "v_mfma_f32_16x16x32_bf16 %[d0], v[232:235], v[152:155], %[d0]\nds_read_b64_tr_b16 v[228:229], %[b0] offset:15424\n" \
  "ds_read_b64_tr_b16 v[230:231], %[b1] offset:15424\nv_max3_u32 %[mx], v152, v154, %[mx]\ns_waitcnt lgkmcnt(12)\n" \
  "v_mfma_f32_16x16x32_bf16 %[d1], v[236:239], v[152:155], %[d1]\nds_read_b64_tr_b16 v[232:233], %[b0] offset:15456\n" \
  "ds_read_b64_tr_b16 v[234:235], %[b1] offset:15456\ns_waitcnt lgkmcnt(12)\nv_mfma_f32_16x16x32_bf16 %[d2], v[208:211], v[152:155], %[d2]\n" \
  "ds_read_b64_tr_b16 v[236:237], %[b0] offset:15488\nds_read_b64_tr_b16 v[238:239], %[b1] offset:15488\ns_waitcnt lgkmcnt(12)\n" \
  "v_mfma_f32_16x16x32_bf16 %[d3], v[212:215], v[152:155], %[d3]\ns_waitcnt lgkmcnt(10)\nv_mfma_f32_16x16x32_bf16 %[d4], v[216:219], v[152:155], %[d4]\n" \
  "s_waitcnt lgkmcnt(8) vmcnt(8)\nv_mfma_f32_16x16x32_bf16 %[d0], v[220:223], v[156:159], %[d0]\nv_max3_u32 %[mx], v156, v158, %[mx]\n" \
  "s_waitcnt lgkmcnt(6)\nv_mfma_f32_16x16x32_bf16 %[d1], v[224:227], v[156:159], %[d1]\ns_waitcnt lgkmcnt(4)\n" \
  "v_mfma_f32_16x16x32_bf16 %[d2], v[228:231], v[156:159], %[d2]\ns_waitcnt lgkmcnt(2)\nv_mfma_f32_16x16x32_bf16 %[d3], v[232:235], v[156:159], %[d3]\n" \
  "s_waitcnt lgkmcnt(0)\nv_mfma_f32_16x16x32_bf16 %[d4], v[236:239], v[156:159], %[d4]\ns_waitcnt vmcnt(0)\nv_max3_u32 %[mx], v160, v162, %[mx]\n" \
  "v_max3_u32 %[mx], v164, v166, %[mx]\nv_max3_u32 %[mx], v168, v170, %[mx]\nv_max3_u32 %[mx], v172, v174, %[mx]\ns_nop 7\n" \
  ""
#define OPROJ_B_ASM \
  "ds_read_b64_tr_b16 v[208:209], %[b0] offset:20480\nds_read_b64_tr_b16 v[210:211], %[b1] offset:20480\n" \
  "ds_read_b64_tr_b16 v[212:213], %[b0] offset:20512\nds_read_b64_tr_b16 v[214:215], %[b1] offset:20512\n" \
  "ds_read_b64_tr_b16 v[216:217], %[b0] offset:20544\nds_read_b64_tr_b16 v[218:219], %[b1] offset:20544\n" \
  "ds_read_b64_tr_b16 v[220:221], %[b0] offset:20576\nds_read_b64_tr_b16 v[222:223], %[b1] offset:20576\n" \
  "ds_read_b64_tr_b16 v[224:225], %[b0] offset:20608\nds_read_b64_tr_b16 v[226:227], %[b1] offset:20608\n" \
  "ds_read_b64_tr_b16 v[228:229], %[b0] offset:25600\nds_read_b64_tr_b16 v[230:231], %[b1] offset:25600\n" \
  "ds_read_b64_tr_b16 v[232:233], %[b0] offset:25632\nds_read_b64_tr_b16 v[234:235], %[b1] offset:25632\ns_waitcnt lgkmcnt(12) vmcnt(6)\n" \
  "v_mfma_f32_16x16x32_bf16 %[d0], v[208:211], v[160:163], %[d0]\nds_read_b64_tr_b16 v[236:237], %[b0] offset:25664\n" \
  "ds_read_b64_tr_b16 v[238:239], %[b1] offset:25664\ns_waitcnt lgkmcnt(12)\nv_mfma_f32_16x16x32_bf16 %[d1], v[212:215], v[160:163], %[d1]\n" \
  "ds_read_b64_tr_b16 v[208:209], %[b0] offset:25696\nds_read_b64_tr_b16 v[210:211], %[b1] offset:25696\ns_waitcnt lgkmcnt(12)\n" \
  "v_mfma_f32_16x16x32_bf16 %[d2], v[216:219], v[160:163], %[d2]\nds_read_b64_tr_b16 v[212:213], %[b0] offset:25728\n" \
  "ds_read_b64_tr_b16 v[214:215], %[b1] offset:25728\ns_waitcnt lgkmcnt(12)\nv_mfma_f32_16x16x32_bf16 %[d3], v[220:223], v[160:163], %[d3]\n" \
  "ds_read_b64_tr_b16 v[216:217], %[b0] offset:30720\nds_read_b64_tr_b16 v[218:219], %[b1] offset:30720\ns_waitcnt lgkmcnt(12)\n" \
  "v_mfma_f32_16x16x32_bf16 %[d4], v[224:227], v[160:163], %[d4]\nds_read_b64_tr_b16 v[220:221], %[b0] offset:30752\n" \
  "ds_read_b64_tr_b16 v[222:223], %[b1] offset:30752\ns_waitcnt lgkmcnt(12) vmcnt(4)\nv_mfma_f32_16x16x32_bf16 %[d0], v[228:231], v[164:167], %[d0]\n" \
  "ds_read_b64_tr_b16 v[224:225], %[b0] offset:30784\nds_read_b64_tr_b16 v[226:227], %[b1] offset:30784\ns_waitcnt lgkmcnt(12)\n" \
  "v_mfma_f32_16x16x32_bf16 %[d1], v[232:235], v[164:167], %[d1]\nds_read_b64_tr_b16 v[228:229], %[b0] offset:30816\n" \
  "ds_read_b64_tr_b16 v[230:231], %[b1] offset:30816\ns_waitcnt lgkmcnt(12)\nv_mfma_f32_16x16x32_bf16 %[d2], v[236:239], v[164:167], %[d2]\n" \
  "ds_read_b64_tr_b16 v[232:233], %[b0] offset:30848\nds_read_b64_tr_b16 v[234:235], %[b1] offset:30848\ns_waitcnt lgkmcnt(12)\n" \
  "v_mfma_f32_16x16x32_bf16 %[d3], v[208:211], v[164:167], %[d3]\nds_read_b64_tr_b16 v[236:237], %[c0] offset:0\n" \
  "ds_read_b64_tr_b16 v[238:239], %[c1] offset:0\ns_waitcnt lgkmcnt(12)\nv_mfma_f32_16x16x32_bf16 %[d4], v[212:215], v[164:167], %[d4]\n" \
  "ds_read_b64_tr_b16 v[208:209], %[c0] offset:32\nds_read_b64_tr_b16 v[210:211], %[c1] offset:32\ns_waitcnt lgkmcnt(12) vmcnt(2)\n" \
  "v_mfma_f32_16x16x32_bf16 %[d0], v[216:219], v[168:171], %[d0]\nds_read_b64_tr_b16 v[212:213], %[c0] offset:64\n" \
  "ds_read_b64_tr_b16 v[214:215], %[c1] offset:64\ns_waitcnt lgkmcnt(12)\nv_mfma_f32_16x16x32_bf16 %[d1], v[220:223], v[168:171], %[d1]\n" \
  "ds_read_b64_tr_b16 v[216:217], %[c0] offset:96\nds_read_b64_tr_b16 v[218:219], %[c1] offset:96\ns_waitcnt lgkmcnt(12)\n" \
  "v_mfma_f32_16x16x32_bf16 %[d2], v[224:227], v[168:171], %[d2]\nds_read_b64_tr_b16 v[220:221], %[c0] offset:128\n" \
  "ds_read_b64_tr_b16 v[222:223], %[c1] offset:128\ns_waitcnt lgkmcnt(12)\nv_mfma_f32_16x16x32_bf16 %[d3], v[228:231], v[168:171], %[d3]\n" \
  "s_waitcnt lgkmcnt(10)\nv_mfma_f32_16x16x32_bf16 %[d4], v[232:235], v[168:171], %[d4]\ns_waitcnt lgkmcnt(8) vmcnt(0)\n" \
  "v_mfma_f32_16x16x32_bf16 %[d0], v[236:239], v[172:175], %[d0]\ns_waitcnt lgkmcnt(6)\nv_mfma_f32_16x16x32_bf16 %[d1], v[208:211], v[172:175], %[d1]\n" \
  "s_waitcnt lgkmcnt(4)\nv_mfma_f32_16x16x32_bf16 %[d2], v[212:215], v[172:175], %[d2]\ns_waitcnt lgkmcnt(2)\n" \
  "v_mfma_f32_16x16x32_bf16 %[d3], v[216:219], v[172:175], %[d3]\ns_waitcnt lgkmcnt(0)\nv_mfma_f32_16x16x32_bf16 %[d4], v[220:223], v[172:175], %[d4]\n" \
  "s_nop 7\n" \
  ""
#define CONV0_ASM \
  "ds_read_b64 v[224:225], %[ga] offset:3776\nds_read_b64 v[226:227], %[ga] offset:3784\nds_read_b64 v[228:229], %[ga] offset:3264\n" \
  "ds_read_b64 v[230:231], %[ga] offset:3272\nds_read_b64 v[232:233], %[ga] offset:2752\nds_read_b64 v[234:235], %[ga] offset:2760\n" \
  "ds_read_b128 v[160:163], %[fa] offset:192\nds_read_b128 v[172:175], %[fa] offset:1728\nds_read_b128 v[176:179], %[fa] offset:2240\n" \
  "ds_read_b128 v[188:191], %[fa] offset:3776\nds_read_b128 v[164:167], %[fa] offset:704\nds_read_b128 v[168:171], %[fa] offset:1216\n" \
  "ds_read_b128 v[180:183], %[fa] offset:2752\nds_read_b128 v[184:187], %[fa] offset:3264\ns_waitcnt lgkmcnt(7)\n" \
  "v_mfma_f32_16x16x32_bf16 %[d0], v[224:227], v[160:163], 0\ns_waitcnt lgkmcnt(6)\nv_mfma_f32_16x16x32_bf16 %[d1], v[224:227], v[172:175], 0\n" \
  "s_waitcnt lgkmcnt(5)\nv_mfma_f32_16x16x32_bf16 %[d2], v[224:227], v[176:179], 0\ns_waitcnt lgkmcnt(4)\n" \
  "v_mfma_f32_16x16x32_bf16 %[d3], v[224:227], v[188:191], 0\nds_read_b64 v[236:237], %[ga] offset:2240\nds_read_b64 v[238:239], %[ga] offset:2248\n" \
  "s_waitcnt lgkmcnt(4)\nv_mfma_f32_16x16x32_bf16 %[d1], v[228:231], v[168:171], %[d1]\nv_mfma_f32_16x16x32_bf16 %[d2], v[228:231], v[172:175], %[d2]\n" \
  "s_waitcnt lgkmcnt(2)\nv_mfma_f32_16x16x32_bf16 %[d3], v[228:231], v[184:187], %[d3]\nds_read_b64 v[224:225], %[ga] offset:1728\n" \
  "ds_read_b64 v[226:227], %[ga] offset:1736\nds_read_b128 v[192:195], %[fa] offset:128\nv_mfma_f32_16x16x32_bf16 %[d1], v[232:235], v[164:167], %[d1]\n" \
  "v_mfma_f32_16x16x32_bf16 %[d2], v[232:235], v[168:171], %[d2]\nv_mfma_f32_16x16x32_bf16 %[d3], v[232:235], v[180:183], %[d3]\n" \
  "ds_read_b64 v[228:229], %[ga] offset:1216\nds_read_b64 v[230:231], %[ga] offset:1224\nds_read_b128 v[204:207], %[fa] offset:1664\n" \
  "s_waitcnt lgkmcnt(6)\nv_mfma_f32_16x16x32_bf16 %[d1], v[236:239], v[160:163], %[d1]\nv_mfma_f32_16x16x32_bf16 %[d2], v[236:239], v[164:167], %[d2]\n" \
  "v_mfma_f32_16x16x32_bf16 %[d3], v[236:239], v[176:179], %[d3]\nds_read_b64 v[232:233], %[ga] offset:704\nds_read_b64 v[234:235], %[ga] offset:712\n" \
  "ds_read_b128 v[208:211], %[fa] offset:2176\ns_waitcnt lgkmcnt(7)\nv_mfma_f32_16x16x32_bf16 %[d2], v[224:227], v[160:163], %[d2]\n" \
  "v_mfma_f32_16x16x32_bf16 %[d3], v[224:227], v[172:175], %[d3]\nds_read_b64 v[236:237], %[ga] offset:192\nds_read_b64 v[238:239], %[ga] offset:200\n" \
  "ds_read_b128 v[220:223], %[fa] offset:3712\ns_waitcnt lgkmcnt(7)\nv_mfma_f32_16x16x32_bf16 %[d3], v[228:231], v[168:171], %[d3]\n" \
  "ds_read_b64 v[224:225], %[ga] offset:3712\nds_read_b64 v[226:227], %[ga] offset:3720\nds_read_b128 v[196:199], %[fa] offset:640\n" \
  "s_waitcnt lgkmcnt(7)\nv_mfma_f32_16x16x32_bf16 %[d3], v[232:235], v[164:167], %[d3]\nds_read_b64 v[228:229], %[ga] offset:3200\n" \
  "ds_read_b64 v[230:231], %[ga] offset:3208\nds_read_b128 v[200:203], %[fa] offset:1152\ns_waitcnt lgkmcnt(7)\n" \
  "v_mfma_f32_16x16x32_bf16 %[d3], v[236:239], v[160:163], %[d3]\nds_read_b64 v[232:233], %[ga] offset:2688\nds_read_b64 v[234:235], %[ga] offset:2696\n" \
  "ds_read_b128 v[212:215], %[fa] offset:2688\nds_read_b128 v[216:219], %[fa] offset:3200\ns_waitcnt lgkmcnt(8)\n" \
  "v_mfma_f32_16x16x32_bf16 %[d0], v[224:227], v[192:195], %[d0]\nv_mfma_f32_16x16x32_bf16 %[d1], v[224:227], v[204:207], %[d1]\n" \
  "v_mfma_f32_16x16x32_bf16 %[d2], v[224:227], v[208:211], %[d2]\nv_mfma_f32_16x16x32_bf16 %[d3], v[224:227], v[220:223], %[d3]\n" \
  "ds_read_b64 v[236:237], %[ga] offset:2176\nds_read_b64 v[238:239], %[ga] offset:2184\ns_waitcnt lgkmcnt(6)\n" \
  "v_mfma_f32_16x16x32_bf16 %[d1], v[228:231], v[200:203], %[d1]\nv_mfma_f32_16x16x32_bf16 %[d2], v[228:231], v[204:207], %[d2]\ns_waitcnt lgkmcnt(2)\n" \
  "v_mfma_f32_16x16x32_bf16 %[d3], v[228:231], v[216:219], %[d3]\nds_read_b64 v[224:225], %[ga] offset:1664\nds_read_b64 v[226:227], %[ga] offset:1672\n" \
  "ds_read_b128 v[160:163], %[fa] offset:64\nv_mfma_f32_16x16x32_bf16 %[d1], v[232:235], v[196:199], %[d1]\n" \
  "v_mfma_f32_16x16x32_bf16 %[d2], v[232:235], v[200:203], %[d2]\nv_mfma_f32_16x16x32_bf16 %[d3], v[232:235], v[212:215], %[d3]\n" \
  "ds_read_b64 v[228:229], %[ga] offset:1152\nds_read_b64 v[230:231], %[ga] offset:1160\nds_read_b128 v[172:175], %[fa] offset:1600\n" \
  "s_waitcnt lgkmcnt(6)\nv_mfma_f32_16x16x32_bf16 %[d1], v[236:239], v[192:195], %[d1]\nv_mfma_f32_16x16x32_bf16 %[d2], v[236:239], v[196:199], %[d2]\n" \
  "v_mfma_f32_16x16x32_bf16 %[d3], v[236:239], v[208:211], %[d3]\nds_read_b64 v[232:233], %[ga] offset:640\nds_read_b64 v[234:235], %[ga] offset:648\n" \
  "ds_read_b128 v[176:179], %[fa] offset:2112\ns_waitcnt lgkmcnt(7)\nv_mfma_f32_16x16x32_bf16 %[d2], v[224:227], v[192:195], %[d2]\n" \
  "v_mfma_f32_16x16x32_bf16 %[d3], v[224:227], v[204:207], %[d3]\nds_read_b64 v[236:237], %[ga] offset:128\nds_read_b64 v[238:239], %[ga] offset:136\n" \
  "ds_read_b128 v[188:191], %[fa] offset:3648\ns_waitcnt lgkmcnt(7)\nv_mfma_f32_16x16x32_bf16 %[d3], v[228:231], v[200:203], %[d3]\n" \
  "ds_read_b64 v[224:225], %[ga] offset:3648\nds_read_b64 v[226:227], %[ga] offset:3656\nds_read_b128 v[164:167], %[fa] offset:576\n" \
  "s_waitcnt lgkmcnt(7)\nv_mfma_f32_16x16x32_bf16 %[d3], v[232:235], v[196:199], %[d3]\nds_read_b64 v[228:229], %[ga] offset:3136\n" \
  "ds_read_b64 v[230:231], %[ga] offset:3144\nds_read_b128 v[168:171], %[fa] offset:1088\ns_waitcnt lgkmcnt(7)\n" \
  "v_mfma_f32_16x16x32_bf16 %[d3], v[236:239], v[192:195], %[d3]\nds_read_b64 v[232:233], %[ga] offset:2624\nds_read_b64 v[234:235], %[ga] offset:2632\n" \
  "ds_read_b128 v[180:183], %[fa] offset:2624\nds_read_b128 v[184:187], %[fa] offset:3136\ns_waitcnt lgkmcnt(8)\n" \
  "v_mfma_f32_16x16x32_bf16 %[d0], v[224:227], v[160:163], %[d0]\nv_mfma_f32_16x16x32_bf16 %[d1], v[224:227], v[172:175], %[d1]\n" \
  "v_mfma_f32_16x16x32_bf16 %[d2], v[224:227], v[176:179], %[d2]\nv_mfma_f32_16x16x32_bf16 %[d3], v[224:227], v[188:191], %[d3]\n" \
  "ds_read_b64 v[236:237], %[ga] offset:2112\nds_read_b64 v[238:239], %[ga] offset:2120\ns_waitcnt lgkmcnt(6)\n" \
  "v_mfma_f32_16x16x32_bf16 %[d1], v[228:231], v[168:171], %[d1]\nv_mfma_f32_16x16x32_bf16 %[d2], v[228:231], v[172:175], %[d2]\ns_waitcnt lgkmcnt(2)\n" \
  "v_mfma_f32_16x16x32_bf16 %[d3], v[228:231], v[184:187], %[d3]\nds_read_b64 v[224:225], %[ga] offset:1600\nds_read_b64 v[226:227], %[ga] offset:1608\n" \
  "ds_read_b128 v[192:195], %[fa] offset:0\nv_mfma_f32_16x16x32_bf16 %[d1], v[232:235], v[164:167], %[d1]\n" \
  "v_mfma_f32_16x16x32_bf16 %[d2], v[232:235], v[168:171], %[d2]\nv_mfma_f32_16x16x32_bf16 %[d3], v[232:235], v[180:183], %[d3]\n" \
  "ds_read_b64 v[228:229], %[ga] offset:1088\nds_read_b64 v[230:231], %[ga] offset:1096\nds_read_b128 v[204:207], %[fa] offset:1536\n" \
  "s_waitcnt lgkmcnt(6)\nv_mfma_f32_16x16x32_bf16 %[d1], v[236:239], v[160:163], %[d1]\nv_mfma_f32_16x16x32_bf16 %[d2], v[236:239], v[164:167], %[d2]\n" \
  "v_mfma_f32_16x16x32_bf16 %[d3], v[236:239], v[176:179], %[d3]\nds_read_b64 v[232:233], %[ga] offset:576\nds_read_b64 v[234:235], %[ga] offset:584\n" \
  "ds_read_b128 v[208:211], %[fa] offset:2048\ns_waitcnt lgkmcnt(7)\nv_mfma_f32_16x16x32_bf16 %[d2], v[224:227], v[160:163], %[d2]\n" \
  "v_mfma_f32_16x16x32_bf16 %[d3], v[224:227], v[172:175], %[d3]\nds_read_b64 v[236:237], %[ga] offset:64\nds_read_b64 v[238:239], %[ga] offset:72\n" \
  "ds_read_b128 v[220:223], %[fa] offset:3584\ns_waitcnt lgkmcnt(7)\nv_mfma_f32_16x16x32_bf16 %[d3], v[228:231], v[168:171], %[d3]\n" \
  "ds_read_b64 v[224:225], %[ga] offset:3584\nds_read_b64 v[226:227], %[ga] offset:3592\nds_read_b128 v[196:199], %[fa] offset:512\n" \
  "s_waitcnt lgkmcnt(7)\nv_mfma_f32_16x16x32_bf16 %[d3], v[232:235], v[164:167], %[d3]\nds_read_b64 v[228:229], %[ga] offset:3072\n" \
  "ds_read_b64 v[230:231], %[ga] offset:3080\nds_read_b128 v[200:203], %[fa] offset:1024\ns_waitcnt lgkmcnt(7)\n" \
  "v_mfma_f32_16x16x32_bf16 %[d3], v[236:239], v[160:163], %[d3]\nds_read_b64 v[232:233], %[ga] offset:2560\nds_read_b64 v[234:235], %[ga] offset:2568\n" \
  "ds_read_b128 v[212:215], %[fa] offset:2560\nds_read_b128 v[216:219], %[fa] offset:3072\ns_waitcnt lgkmcnt(8)\n" \
  "v_mfma_f32_16x16x32_bf16 %[d0], v[224:227], v[192:195], %[d0]\nv_mfma_f32_16x16x32_bf16 %[d1], v[224:227], v[204:207], %[d1]\n" \
  "v_mfma_f32_16x16x32_bf16 %[d2], v[224:227], v[208:211], %[d2]\nv_mfma_f32_16x16x32_bf16 %[d3], v[224:227], v[220:223], %[d3]\n" \
  "ds_read_b64 v[236:237], %[ga] offset:2048\nds_read_b64 v[238:239], %[ga] offset:2056\ns_waitcnt lgkmcnt(6)\n" \
  "v_mfma_f32_16x16x32_bf16 %[d1], v[228:231], v[200:203], %[d1]\nv_mfma_f32_16x16x32_bf16 %[d2], v[228:231], v[204:207], %[d2]\ns_waitcnt lgkmcnt(2)\n" \
  "v_mfma_f32_16x16x32_bf16 %[d3], v[228:231], v[216:219], %[d3]\nds_read_b64 v[224:225], %[ga] offset:1536\nds_read_b64 v[226:227], %[ga] offset:1544\n" \
  "v_mfma_f32_16x16x32_bf16 %[d1], v[232:235], v[196:199], %[d1]\nv_mfma_f32_16x16x32_bf16 %[d2], v[232:235], v[200:203], %[d2]\n" \
  "v_mfma_f32_16x16x32_bf16 %[d3], v[232:235], v[212:215], %[d3]\nds_read_b64 v[228:229], %[ga] offset:1024\nds_read_b64 v[230:231], %[ga] offset:1032\n" \
  "s_waitcnt lgkmcnt(4)\nv_mfma_f32_16x16x32_bf16 %[d1], v[236:239], v[192:195], %[d1]\nv_mfma_f32_16x16x32_bf16 %[d2], v[236:239], v[196:199], %[d2]\n" \
  "v_mfma_f32_16x16x32_bf16 %[d3], v[236:239], v[208:211], %[d3]\nds_read_b64 v[232:233], %[ga] offset:512\nds_read_b64 v[234:235], %[ga] offset:520\n" \
  "s_waitcnt lgkmcnt(4)\nv_mfma_f32_16x16x32_bf16 %[d2], v[224:227], v[192:195], %[d2]\nv_mfma_f32_16x16x32_bf16 %[d3], v[224:227], v[204:207], %[d3]\n" \
  "ds_read_b64 v[236:237], %[ga] offset:0\nds_read_b64 v[238:239], %[ga] offset:8\ns_waitcnt lgkmcnt(4)\n" \
  "v_mfma_f32_16x16x32_bf16 %[d3], v[228:231], v[200:203], %[d3]\ns_waitcnt lgkmcnt(2)\nv_mfma_f32_16x16x32_bf16 %[d3], v[232:235], v[196:199], %[d3]\n" \
  "s_waitcnt lgkmcnt(0)\nv_mfma_f32_16x16x32_bf16 %[d3], v[236:239], v[192:195], %[d3]\ns_nop 7\n" \
  ""
#define CONV1_ASM \
  "ds_read_b64 v[224:225], %[ga] offset:3776\nds_read_b64 v[226:227], %[ga] offset:3784\nds_read_b64 v[228:229], %[ga] offset:3264\n" \
  "ds_read_b64 v[230:231], %[ga] offset:3272\nds_read_b64 v[232:233], %[ga] offset:2752\nds_read_b64 v[234:235], %[ga] offset:2760\n" \
  "ds_read_b128 v[164:167], %[fa] offset:704\nds_read_b128 v[168:171], %[fa] offset:1216\nds_read_b128 v[180:183], %[fa] offset:2752\n" \
  "ds_read_b128 v[184:187], %[fa] offset:3264\nds_read_b128 v[160:163], %[fa] offset:192\nds_read_b128 v[172:175], %[fa] offset:1728\n" \
  "ds_read_b128 v[176:179], %[fa] offset:2240\ns_waitcnt lgkmcnt(6)\nv_mfma_f32_16x16x32_bf16 %[d0], v[224:227], v[164:167], 0\ns_waitcnt lgkmcnt(5)\n" \
  "v_mfma_f32_16x16x32_bf16 %[d1], v[224:227], v[168:171], 0\ns_waitcnt lgkmcnt(4)\nv_mfma_f32_16x16x32_bf16 %[d2], v[224:227], v[180:183], 0\n" \
  "s_waitcnt lgkmcnt(3)\nv_mfma_f32_16x16x32_bf16 %[d3], v[224:227], v[184:187], 0\nds_read_b64 v[236:237], %[ga] offset:2240\n" \
  "ds_read_b64 v[238:239], %[ga] offset:2248\ns_waitcnt lgkmcnt(4)\nv_mfma_f32_16x16x32_bf16 %[d0], v[228:231], v[160:163], %[d0]\n" \
  "v_mfma_f32_16x16x32_bf16 %[d1], v[228:231], v[164:167], %[d1]\ns_waitcnt lgkmcnt(2)\nv_mfma_f32_16x16x32_bf16 %[d2], v[228:231], v[176:179], %[d2]\n" \
  "v_mfma_f32_16x16x32_bf16 %[d3], v[228:231], v[180:183], %[d3]\nds_read_b64 v[224:225], %[ga] offset:1728\nds_read_b64 v[226:227], %[ga] offset:1736\n" \
  "ds_read_b128 v[196:199], %[fa] offset:640\nv_mfma_f32_16x16x32_bf16 %[d1], v[232:235], v[160:163], %[d1]\n" \
  "v_mfma_f32_16x16x32_bf16 %[d2], v[232:235], v[172:175], %[d2]\nv_mfma_f32_16x16x32_bf16 %[d3], v[232:235], v[176:179], %[d3]\n" \
  "ds_read_b64 v[228:229], %[ga] offset:1216\nds_read_b64 v[230:231], %[ga] offset:1224\nds_read_b128 v[200:203], %[fa] offset:1152\n" \
  "s_waitcnt lgkmcnt(6)\nv_mfma_f32_16x16x32_bf16 %[d2], v[236:239], v[168:171], %[d2]\nv_mfma_f32_16x16x32_bf16 %[d3], v[236:239], v[172:175], %[d3]\n" \
  "ds_read_b64 v[232:233], %[ga] offset:704\nds_read_b64 v[234:235], %[ga] offset:712\nds_read_b128 v[212:215], %[fa] offset:2688\ns_waitcnt lgkmcnt(7)\n" \
  "v_mfma_f32_16x16x32_bf16 %[d2], v[224:227], v[164:167], %[d2]\nv_mfma_f32_16x16x32_bf16 %[d3], v[224:227], v[168:171], %[d3]\n" \
  "ds_read_b64 v[236:237], %[ga] offset:3712\nds_read_b64 v[238:239], %[ga] offset:3720\nds_read_b128 v[216:219], %[fa] offset:3200\n" \
  "s_waitcnt lgkmcnt(7)\nv_mfma_f32_16x16x32_bf16 %[d2], v[228:231], v[160:163], %[d2]\nv_mfma_f32_16x16x32_bf16 %[d3], v[228:231], v[164:167], %[d3]\n" \
  "ds_read_b64 v[224:225], %[ga] offset:3200\nds_read_b64 v[226:227], %[ga] offset:3208\nds_read_b128 v[192:195], %[fa] offset:128\n" \
  "s_waitcnt lgkmcnt(7)\nv_mfma_f32_16x16x32_bf16 %[d3], v[232:235], v[160:163], %[d3]\nds_read_b64 v[228:229], %[ga] offset:2688\n" \
  "ds_read_b64 v[230:231], %[ga] offset:2696\nds_read_b128 v[204:207], %[fa] offset:1664\nds_read_b128 v[208:211], %[fa] offset:2176\n" \
  "s_waitcnt lgkmcnt(8)\nv_mfma_f32_16x16x32_bf16 %[d0], v[236:239], v[196:199], %[d0]\nv_mfma_f32_16x16x32_bf16 %[d1], v[236:239], v[200:203], %[d1]\n" \
  "v_mfma_f32_16x16x32_bf16 %[d2], v[236:239], v[212:215], %[d2]\ns_waitcnt lgkmcnt(7)\nv_mfma_f32_16x16x32_bf16 %[d3], v[236:239], v[216:219], %[d3]\n" \
  "ds_read_b64 v[232:233], %[ga] offset:2176\nds_read_b64 v[234:235], %[ga] offset:2184\ns_waitcnt lgkmcnt(6)\n" \
  "v_mfma_f32_16x16x32_bf16 %[d0], v[224:227], v[192:195], %[d0]\nv_mfma_f32_16x16x32_bf16 %[d1], v[224:227], v[196:199], %[d1]\ns_waitcnt lgkmcnt(2)\n" \
  "v_mfma_f32_16x16x32_bf16 %[d2], v[224:227], v[208:211], %[d2]\nv_mfma_f32_16x16x32_bf16 %[d3], v[224:227], v[212:215], %[d3]\n" \
  "ds_read_b64 v[236:237], %[ga] offset:1664\nds_read_b64 v[238:239], %[ga] offset:1672\nds_read_b128 v[164:167], %[fa] offset:576\n" \
  "v_mfma_f32_16x16x32_bf16 %[d1], v[228:231], v[192:195], %[d1]\nv_mfma_f32_16x16x32_bf16 %[d2], v[228:231], v[204:207], %[d2]\n" \
  "v_mfma_f32_16x16x32_bf16 %[d3], v[228:231], v[208:211], %[d3]\nds_read_b64 v[224:225], %[ga] offset:1152\nds_read_b64 v[226:227], %[ga] offset:1160\n" \
  "ds_read_b128 v[168:171], %[fa] offset:1088\ns_waitcnt lgkmcnt(6)\nv_mfma_f32_16x16x32_bf16 %[d2], v[232:235], v[200:203], %[d2]\n" \
  "v_mfma_f32_16x16x32_bf16 %[d3], v[232:235], v[204:207], %[d3]\nds_read_b64 v[228:229], %[ga] offset:640\nds_read_b64 v[230:231], %[ga] offset:648\n" \
  "ds_read_b128 v[180:183], %[fa] offset:2624\ns_waitcnt lgkmcnt(7)\nv_mfma_f32_16x16x32_bf16 %[d2], v[236:239], v[196:199], %[d2]\n" \
  "v_mfma_f32_16x16x32_bf16 %[d3], v[236:239], v[200:203], %[d3]\nds_read_b64 v[232:233], %[ga] offset:3648\nds_read_b64 v[234:235], %[ga] offset:3656\n" \
  "ds_read_b128 v[184:187], %[fa] offset:3136\ns_waitcnt lgkmcnt(7)\nv_mfma_f32_16x16x32_bf16 %[d2], v[224:227], v[192:195], %[d2]\n" \
  "v_mfma_f32_16x16x32_bf16 %[d3], v[224:227], v[196:199], %[d3]\nds_read_b64 v[236:237], %[ga] offset:3136\nds_read_b64 v[238:239], %[ga] offset:3144\n" \
  "ds_read_b128 v[160:163], %[fa] offset:64\ns_waitcnt lgkmcnt(7)\nv_mfma_f32_16x16x32_bf16 %[d3], v[228:231], v[192:195], %[d3]\n" \
  "ds_read_b64 v[224:225], %[ga] offset:2624\nds_read_b64 v[226:227], %[ga] offset:2632\nds_read_b128 v[172:175], %[fa] offset:1600\n" \
  "ds_read_b128 v[176:179], %[fa] offset:2112\ns_waitcnt lgkmcnt(8)\nv_mfma_f32_16x16x32_bf16 %[d0], v[232:235], v[164:167], %[d0]\n" \
  "v_mfma_f32_16x16x32_bf16 %[d1], v[232:235], v[168:171], %[d1]\nv_mfma_f32_16x16x32_bf16 %[d2], v[232:235], v[180:183], %[d2]\ns_waitcnt lgkmcnt(7)\n" \
  "v_mfma_f32_16x16x32_bf16 %[d3], v[232:235], v[184:187], %[d3]\nds_read_b64 v[228:229], %[ga] offset:2112\nds_read_b64 v[230:231], %[ga] offset:2120\n" \
  "s_waitcnt lgkmcnt(6)\nv_mfma_f32_16x16x32_bf16 %[d0], v[236:239], v[160:163], %[d0]\nv_mfma_f32_16x16x32_bf16 %[d1], v[236:239], v[164:167], %[d1]\n" \
  "s_waitcnt lgkmcnt(2)\nv_mfma_f32_16x16x32_bf16 %[d2], v[236:239], v[176:179], %[d2]\nv_mfma_f32_16x16x32_bf16 %[d3], v[236:239], v[180:183], %[d3]\n" \
  "ds_read_b64 v[232:233], %[ga] offset:1600\nds_read_b64 v[234:235], %[ga] offset:1608\nds_read_b128 v[196:199], %[fa] offset:512\n" \
  "v_mfma_f32_16x16x32_bf16 %[d1], v[224:227], v[160:163], %[d1]\nv_mfma_f32_16x16x32_bf16 %[d2], v[224:227], v[172:175], %[d2]\n" \
  "v_mfma_f32_16x16x32_bf16 %[d3], v[224:227], v[176:179], %[d3]\nds_read_b64 v[236:237], %[ga] offset:1088\nds_read_b64 v[238:239], %[ga] offset:1096\n" \
  "ds_read_b128 v[200:203], %[fa] offset:1024\ns_waitcnt lgkmcnt(6)\nv_mfma_f32_16x16x32_bf16 %[d2], v[228:231], v[168:171], %[d2]\n" \
  "v_mfma_f32_16x16x32_bf16 %[d3], v[228:231], v[172:175], %[d3]\nds_read_b64 v[224:225], %[ga] offset:576\nds_read_b64 v[226:227], %[ga] offset:584\n" \
  "ds_read_b128 v[212:215], %[fa] offset:2560\ns_waitcnt lgkmcnt(7)\nv_mfma_f32_16x16x32_bf16 %[d2], v[232:235], v[164:167], %[d2]\n" \
  "v_mfma_f32_16x16x32_bf16 %[d3], v[232:235], v[168:171], %[d3]\nds_read_b64 v[228:229], %[ga] offset:3584\nds_read_b64 v[230:231], %[ga] offset:3592\n" \
  "ds_read_b128 v[216:219], %[fa] offset:3072\ns_waitcnt lgkmcnt(7)\nv_mfma_f32_16x16x32_bf16 %[d2], v[236:239], v[160:163], %[d2]\n" \
  "v_mfma_f32_16x16x32_bf16 %[d3], v[236:239], v[164:167], %[d3]\nds_read_b64 v[232:233], %[ga] offset:3072\nds_read_b64 v[234:235], %[ga] offset:3080\n" \
  "ds_read_b128 v[192:195], %[fa] offset:0\ns_waitcnt lgkmcnt(7)\nv_mfma_f32_16x16x32_bf16 %[d3], v[224:227], v[160:163], %[d3]\n" \
  "ds_read_b64 v[236:237], %[ga] offset:2560\nds_read_b64 v[238:239], %[ga] offset:2568\nds_read_b128 v[204:207], %[fa] offset:1536\n" \
  "ds_read_b128 v[208:211], %[fa] offset:2048\ns_waitcnt lgkmcnt(8)\nv_mfma_f32_16x16x32_bf16 %[d0], v[228:231], v[196:199], %[d0]\n" \
  "v_mfma_f32_16x16x32_bf16 %[d1], v[228:231], v[200:203], %[d1]\nv_mfma_f32_16x16x32_bf16 %[d2], v[228:231], v[212:215], %[d2]\ns_waitcnt lgkmcnt(7)\n" \
  "v_mfma_f32_16x16x32_bf16 %[d3], v[228:231], v[216:219], %[d3]\nds_read_b64 v[224:225], %[ga] offset:2048\nds_read_b64 v[226:227], %[ga] offset:2056\n" \
  "s_waitcnt lgkmcnt(6)\nv_mfma_f32_16x16x32_bf16 %[d0], v[232:235], v[192:195], %[d0]\nv_mfma_f32_16x16x32_bf16 %[d1], v[232:235], v[196:199], %[d1]\n" \
  "s_waitcnt lgkmcnt(2)\nv_mfma_f32_16x16x32_bf16 %[d2], v[232:235], v[208:211], %[d2]\nv_mfma_f32_16x16x32_bf16 %[d3], v[232:235], v[212:215], %[d3]\n" \
  "ds_read_b64 v[228:229], %[ga] offset:1536\nds_read_b64 v[230:231], %[ga] offset:1544\nv_mfma_f32_16x16x32_bf16 %[d1], v[236:239], v[192:195], %[d1]\n" \
  "v_mfma_f32_16x16x32_bf16 %[d2], v[236:239], v[204:207], %[d2]\nv_mfma_f32_16x16x32_bf16 %[d3], v[236:239], v[208:211], %[d3]\n" \
  "ds_read_b64 v[232:233], %[ga] offset:1024\nds_read_b64 v[234:235], %[ga] offset:1032\ns_waitcnt lgkmcnt(4)\n" \
  "v_mfma_f32_16x16x32_bf16 %[d2], v[224:227], v[200:203], %[d2]\nv_mfma_f32_16x16x32_bf16 %[d3], v[224:227], v[204:207], %[d3]\n" \
  "ds_read_b64 v[236:237], %[ga] offset:512\nds_read_b64 v[238:239], %[ga] offset:520\ns_waitcnt lgkmcnt(4)\n" \
  "v_mfma_f32_16x16x32_bf16 %[d2], v[228:231], v[196:199], %[d2]\nv_mfma_f32_16x16x32_bf16 %[d3], v[228:231], v[200:203], %[d3]\ns_waitcnt lgkmcnt(2)\n" \
  "v_mfma_f32_16x16x32_bf16 %[d2], v[232:235], v[192:195], %[d2]\nv_mfma_f32_16x16x32_bf16 %[d3], v[232:235], v[196:199], %[d3]\ns_waitcnt lgkmcnt(0)\n" \
  "v_mfma_f32_16x16x32_bf16 %[d3], v[236:239], v[192:195], %[d3]\ns_nop 7\n" \
  ""
#define CONV0D_ASM \
  "ds_read_b64 v[224:225], %[ga] offset:3776\nds_read_b64 v[226:227], %[ga] offset:3784\nds_read_b64 v[228:229], %[ga] offset:3264\n" \
  "ds_read_b64 v[230:231], %[ga] offset:3272\nds_read_b64 v[232:233], %[ga] offset:2752\nds_read_b64 v[234:235], %[ga] offset:2760\n" \
  "ds_read_b128 v[160:163], %[fa] offset:192\nds_read_b128 v[172:175], %[fa] offset:1728\nds_read_b128 v[176:179], %[fa] offset:2240\n" \
  "ds_read_b128 v[188:191], %[fa] offset:3776\nds_read_b128 v[164:167], %[fa] offset:704\nds_read_b128 v[168:171], %[fa] offset:1216\n" \
  "ds_read_b128 v[180:183], %[fa] offset:2752\nds_read_b128 v[184:187], %[fa] offset:3264\ns_waitcnt lgkmcnt(7)\n" \
  "v_mfma_f32_16x16x32_bf16 %[d0], v[224:227], v[160:163], 0\ns_waitcnt lgkmcnt(6)\nv_mfma_f32_16x16x32_bf16 %[d1], v[224:227], v[172:175], 0\n" \
  "s_waitcnt lgkmcnt(5)\nv_mfma_f32_16x16x32_bf16 %[d2], v[224:227], v[176:179], 0\ns_mov_b32 m0, %[m0]\ns_nop 0\nglobal_load_lds_dwordx4 %[w0], %[wb0]\n" \
  "s_waitcnt lgkmcnt(4)\nv_mfma_f32_16x16x32_bf16 %[d3], v[224:227], v[188:191], 0\nds_read_b64 v[236:237], %[ga] offset:2240\n" \
  "ds_read_b64 v[238:239], %[ga] offset:2248\ns_waitcnt lgkmcnt(4)\nv_mfma_f32_16x16x32_bf16 %[d1], v[228:231], v[168:171], %[d1]\n" \
  "v_mfma_f32_16x16x32_bf16 %[d2], v[228:231], v[172:175], %[d2]\ns_addk_i32 m0, 1024\ns_nop 0\nglobal_load_lds_dwordx4 %[w1], %[wb0]\n" \
  "s_waitcnt lgkmcnt(2)\nv_mfma_f32_16x16x32_bf16 %[d3], v[228:231], v[184:187], %[d3]\nds_read_b64 v[224:225], %[ga] offset:1728\n" \
  "ds_read_b64 v[226:227], %[ga] offset:1736\nds_read_b128 v[192:195], %[fa] offset:128\nv_mfma_f32_16x16x32_bf16 %[d1], v[232:235], v[164:167], %[d1]\n" \
  "v_mfma_f32_16x16x32_bf16 %[d2], v[232:235], v[168:171], %[d2]\ns_addk_i32 m0, 1024\ns_nop 0\nglobal_load_lds_dwordx4 %[w2], %[wb0]\n" \
  "v_mfma_f32_16x16x32_bf16 %[d3], v[232:235], v[180:183], %[d3]\nds_read_b64 v[228:229], %[ga] offset:1216\nds_read_b64 v[230:231], %[ga] offset:1224\n" \
  "ds_read_b128 v[204:207], %[fa] offset:1664\ns_waitcnt lgkmcnt(6)\nv_mfma_f32_16x16x32_bf16 %[d1], v[236:239], v[160:163], %[d1]\n" \
  "v_mfma_f32_16x16x32_bf16 %[d2], v[236:239], v[164:167], %[d2]\ns_addk_i32 m0, 1024\ns_nop 0\nglobal_load_lds_dwordx4 %[w3], %[wb0]\n" \
  "v_mfma_f32_16x16x32_bf16 %[d3], v[236:239], v[176:179], %[d3]\nds_read_b64 v[232:233], %[ga] offset:704\nds_read_b64 v[234:235], %[ga] offset:712\n" \
  "ds_read_b128 v[208:211], %[fa] offset:2176\ns_waitcnt lgkmcnt(7)\nv_mfma_f32_16x16x32_bf16 %[d2], v[224:227], v[160:163], %[d2]\n" \
  "v_mfma_f32_16x16x32_bf16 %[d3], v[224:227], v[172:175], %[d3]\ns_addk_i32 m0, 1024\ns_nop 0\nglobal_load_lds_dwordx4 %[w4], %[wb0]\n" \
  "ds_read_b64 v[236:237], %[ga] offset:192\nds_read_b64 v[238:239], %[ga] offset:200\nds_read_b128 v[220:223], %[fa] offset:3712\ns_waitcnt lgkmcnt(7)\n" \
  "v_mfma_f32_16x16x32_bf16 %[d3], v[228:231], v[168:171], %[d3]\nds_read_b64 v[224:225], %[ga] offset:3712\nds_read_b64 v[226:227], %[ga] offset:3720\n" \
  "ds_read_b128 v[196:199], %[fa] offset:640\ns_waitcnt lgkmcnt(7)\nv_mfma_f32_16x16x32_bf16 %[d3], v[232:235], v[164:167], %[d3]\n" \
  "ds_read_b64 v[228:229], %[ga] offset:3200\nds_read_b64 v[230:231], %[ga] offset:3208\nds_read_b128 v[200:203], %[fa] offset:1152\n" \
  "s_waitcnt lgkmcnt(7)\nv_mfma_f32_16x16x32_bf16 %[d3], v[236:239], v[160:163], %[d3]\ns_mov_b32 m0, %[m1]\ns_nop 0\n" \
  "global_load_lds_dwordx4 %[w0], %[wb1]\nds_read_b64 v[232:233], %[ga] offset:2688\nds_read_b64 v[234:235], %[ga] offset:2696\n" \
  "ds_read_b128 v[212:215], %[fa] offset:2688\nds_read_b128 v[216:219], %[fa] offset:3200\ns_waitcnt lgkmcnt(8)\n" \
  "v_mfma_f32_16x16x32_bf16 %[d0], v[224:227], v[192:195], %[d0]\nv_mfma_f32_16x16x32_bf16 %[d1], v[224:227], v[204:207], %[d1]\n" \
  "v_mfma_f32_16x16x32_bf16 %[d2], v[224:227], v[208:211], %[d2]\ns_addk_i32 m0, 1024\ns_nop 0\nglobal_load_lds_dwordx4 %[w1], %[wb1]\n" \
  "v_mfma_f32_16x16x32_bf16 %[d3], v[224:227], v[220:223], %[d3]\nds_read_b64 v[236:237], %[ga] offset:2176\nds_read_b64 v[238:239], %[ga] offset:2184\n" \
  "s_waitcnt lgkmcnt(6)\nv_mfma_f32_16x16x32_bf16 %[d1], v[228:231], v[200:203], %[d1]\nv_mfma_f32_16x16x32_bf16 %[d2], v[228:231], v[204:207], %[d2]\n" \
  "s_addk_i32 m0, 1024\ns_nop 0\nglobal_load_lds_dwordx4 %[w2], %[wb1]\ns_waitcnt lgkmcnt(2)\n" \
  "v_mfma_f32_16x16x32_bf16 %[d3], v[228:231], v[216:219], %[d3]\nds_read_b64 v[224:225], %[ga] offset:1664\nds_read_b64 v[226:227], %[ga] offset:1672\n" \
  "ds_read_b128 v[160:163], %[fa] offset:64\nv_mfma_f32_16x16x32_bf16 %[d1], v[232:235], v[196:199], %[d1]\n" \
  "v_mfma_f32_16x16x32_bf16 %[d2], v[232:235], v[200:203], %[d2]\ns_addk_i32 m0, 1024\ns_nop 0\nglobal_load_lds_dwordx4 %[w3], %[wb1]\n" \
  "v_mfma_f32_16x16x32_bf16 %[d3], v[232:235], v[212:215], %[d3]\nds_read_b64 v[228:229], %[ga] offset:1152\nds_read_b64 v[230:231], %[ga] offset:1160\n" \
  "ds_read_b128 v[172:175], %[fa] offset:1600\ns_waitcnt lgkmcnt(6)\nv_mfma_f32_16x16x32_bf16 %[d1], v[236:239], v[192:195], %[d1]\n" \
  "v_mfma_f32_16x16x32_bf16 %[d2], v[236:239], v[196:199], %[d2]\ns_addk_i32 m0, 1024\ns_nop 0\nglobal_load_lds_dwordx4 %[w4], %[wb1]\n" \
  "v_mfma_f32_16x16x32_bf16 %[d3], v[236:239], v[208:211], %[d3]\nds_read_b64 v[232:233], %[ga] offset:640\nds_read_b64 v[234:235], %[ga] offset:648\n" \
  "ds_read_b128 v[176:179], %[fa] offset:2112\ns_waitcnt lgkmcnt(7)\nv_mfma_f32_16x16x32_bf16 %[d2], v[224:227], v[192:195], %[d2]\n" \
  "v_mfma_f32_16x16x32_bf16 %[d3], v[224:227], v[204:207], %[d3]\ns_mov_b32 m0, %[m2]\ns_nop 0\nglobal_load_lds_dwordx4 %[w0], %[wb2]\n" \
  "ds_read_b64 v[236:237], %[ga] offset:128\nds_read_b64 v[238:239], %[ga] offset:136\nds_read_b128 v[188:191], %[fa] offset:3648\ns_waitcnt lgkmcnt(7)\n" \
  "v_mfma_f32_16x16x32_bf16 %[d3], v[228:231], v[200:203], %[d3]\nds_read_b64 v[224:225], %[ga] offset:3648\nds_read_b64 v[226:227], %[ga] offset:3656\n" \
  "ds_read_b128 v[164:167], %[fa] offset:576\ns_waitcnt lgkmcnt(7)\nv_mfma_f32_16x16x32_bf16 %[d3], v[232:235], v[196:199], %[d3]\n" \
  "ds_read_b64 v[228:229], %[ga] offset:3136\nds_read_b64 v[230:231], %[ga] offset:3144\nds_read_b128 v[168:171], %[fa] offset:1088\n" \
  "s_waitcnt lgkmcnt(7)\nv_mfma_f32_16x16x32_bf16 %[d3], v[236:239], v[192:195], %[d3]\ns_addk_i32 m0, 1024\ns_nop 0\n" \
  "global_load_lds_dwordx4 %[w1], %[wb2]\nds_read_b64 v[232:233], %[ga] offset:2624\nds_read_b64 v[234:235], %[ga] offset:2632\n" \
  "ds_read_b128 v[180:183], %[fa] offset:2624\nds_read_b128 v[184:187], %[fa] offset:3136\ns_waitcnt lgkmcnt(8)\n" \
  "v_mfma_f32_16x16x32_bf16 %[d0], v[224:227], v[160:163], %[d0]\nv_mfma_f32_16x16x32_bf16 %[d1], v[224:227], v[172:175], %[d1]\n" \
  "v_mfma_f32_16x16x32_bf16 %[d2], v[224:227], v[176:179], %[d2]\ns_addk_i32 m0, 1024\ns_nop 0\nglobal_load_lds_dwordx4 %[w2], %[wb2]\n" \
  "v_mfma_f32_16x16x32_bf16 %[d3], v[224:227], v[188:191], %[d3]\nds_read_b64 v[236:237], %[ga] offset:2112\nds_read_b64 v[238:239], %[ga] offset:2120\n" \
  "s_waitcnt lgkmcnt(6)\nv_mfma_f32_16x16x32_bf16 %[d1], v[228:231], v[168:171], %[d1]\nv_mfma_f32_16x16x32_bf16 %[d2], v[228:231], v[172:175], %[d2]\n" \
  "s_addk_i32 m0, 1024\ns_nop 0\nglobal_load_lds_dwordx4 %[w3], %[wb2]\ns_waitcnt lgkmcnt(2)\n" \
  "v_mfma_f32_16x16x32_bf16 %[d3], v[228:231], v[184:187], %[d3]\nds_read_b64 v[224:225], %[ga] offset:1600\nds_read_b64 v[226:227], %[ga] offset:1608\n" \
  "ds_read_b128 v[192:195], %[fa] offset:0\nv_mfma_f32_16x16x32_bf16 %[d1], v[232:235], v[164:167], %[d1]\n" \
  "v_mfma_f32_16x16x32_bf16 %[d2], v[232:235], v[168:171], %[d2]\ns_addk_i32 m0, 1024\ns_nop 0\nglobal_load_lds_dwordx4 %[w4], %[wb2]\n" \
  "v_mfma_f32_16x16x32_bf16 %[d3], v[232:235], v[180:183], %[d3]\nds_read_b64 v[228:229], %[ga] offset:1088\nds_read_b64 v[230:231], %[ga] offset:1096\n" \
  "ds_read_b128 v[204:207], %[fa] offset:1536\ns_waitcnt lgkmcnt(6)\nv_mfma_f32_16x16x32_bf16 %[d1], v[236:239], v[160:163], %[d1]\n" \
  "v_mfma_f32_16x16x32_bf16 %[d2], v[236:239], v[164:167], %[d2]\ns_mov_b32 m0, %[m3]\ns_nop 0\nglobal_load_lds_dwordx4 %[w0], %[wb3]\n" \
  "v_mfma_f32_16x16x32_bf16 %[d3], v[236:239], v[176:179], %[d3]\nds_read_b64 v[232:233], %[ga] offset:576\nds_read_b64 v[234:235], %[ga] offset:584\n" \
  "ds_read_b128 v[208:211], %[fa] offset:2048\ns_waitcnt lgkmcnt(7)\nv_mfma_f32_16x16x32_bf16 %[d2], v[224:227], v[160:163], %[d2]\n" \
  "v_mfma_f32_16x16x32_bf16 %[d3], v[224:227], v[172:175], %[d3]\ns_addk_i32 m0, 1024\ns_nop 0\nglobal_load_lds_dwordx4 %[w1], %[wb3]\n" \
  "ds_read_b64 v[236:237], %[ga] offset:64\nds_read_b64 v[238:239], %[ga] offset:72\nds_read_b128 v[220:223], %[fa] offset:3584\ns_waitcnt lgkmcnt(7)\n" \
  "v_mfma_f32_16x16x32_bf16 %[d3], v[228:231], v[168:171], %[d3]\nds_read_b64 v[224:225], %[ga] offset:3584\nds_read_b64 v[226:227], %[ga] offset:3592\n" \
  "ds_read_b128 v[196:199], %[fa] offset:512\ns_waitcnt lgkmcnt(7)\nv_mfma_f32_16x16x32_bf16 %[d3], v[232:235], v[164:167], %[d3]\n" \
  "ds_read_b64 v[228:229], %[ga] offset:3072\nds_read_b64 v[230:231], %[ga] offset:3080\nds_read_b128 v[200:203], %[fa] offset:1024\n" \
  "s_waitcnt lgkmcnt(7)\nv_mfma_f32_16x16x32_bf16 %[d3], v[236:239], v[160:163], %[d3]\ns_addk_i32 m0, 1024\ns_nop 0\n" \
  "global_load_lds_dwordx4 %[w2], %[wb3]\nds_read_b64 v[232:233], %[ga] offset:2560\nds_read_b64 v[234:235], %[ga] offset:2568\n" \
  "ds_read_b128 v[212:215], %[fa] offset:2560\nds_read_b128 v[216:219], %[fa] offset:3072\ns_waitcnt lgkmcnt(8)\n" \
  "v_mfma_f32_16x16x32_bf16 %[d0], v[224:227], v[192:195], %[d0]\nv_mfma_f32_16x16x32_bf16 %[d1], v[224:227], v[204:207], %[d1]\n" \
  "v_mfma_f32_16x16x32_bf16 %[d2], v[224:227], v[208:211], %[d2]\ns_addk_i32 m0, 1024\ns_nop 0\nglobal_load_lds_dwordx4 %[w3], %[wb3]\n" \
  "v_mfma_f32_16x16x32_bf16 %[d3], v[224:227], v[220:223], %[d3]\nds_read_b64 v[236:237], %[ga] offset:2048\nds_read_b64 v[238:239], %[ga] offset:2056\n" \
  "s_waitcnt lgkmcnt(6)\nv_mfma_f32_16x16x32_bf16 %[d1], v[228:231], v[200:203], %[d1]\nv_mfma_f32_16x16x32_bf16 %[d2], v[228:231], v[204:207], %[d2]\n" \
  "s_addk_i32 m0, 1024\ns_nop 0\nglobal_load_lds_dwordx4 %[w4], %[wb3]\ns_waitcnt lgkmcnt(2)\n" \
  "v_mfma_f32_16x16x32_bf16 %[d3], v[228:231], v[216:219], %[d3]\nds_read_b64 v[224:225], %[ga] offset:1536\nds_read_b64 v[226:227], %[ga] offset:1544\n" \
  "v_mfma_f32_16x16x32_bf16 %[d1], v[232:235], v[196:199], %[d1]\nv_mfma_f32_16x16x32_bf16 %[d2], v[232:235], v[200:203], %[d2]\n" \
  "v_mfma_f32_16x16x32_bf16 %[d3], v[232:235], v[212:215], %[d3]\nds_read_b64 v[228:229], %[ga] offset:1024\nds_read_b64 v[230:231], %[ga] offset:1032\n" \
  "s_waitcnt lgkmcnt(4)\nv_mfma_f32_16x16x32_bf16 %[d1], v[236:239], v[192:195], %[d1]\nv_mfma_f32_16x16x32_bf16 %[d2], v[236:239], v[196:199], %[d2]\n" \
  "v_mfma_f32_16x16x32_bf16 %[d3], v[236:239], v[208:211], %[d3]\nds_read_b64 v[232:233], %[ga] offset:512\nds_read_b64 v[234:235], %[ga] offset:520\n" \
  "s_waitcnt lgkmcnt(4)\nv_mfma_f32_16x16x32_bf16 %[d2], v[224:227], v[192:195], %[d2]\nv_mfma_f32_16x16x32_bf16 %[d3], v[224:227], v[204:207], %[d3]\n" \
  "ds_read_b64 v[236:237], %[ga] offset:0\nds_read_b64 v[238:239], %[ga] offset:8\ns_waitcnt lgkmcnt(4)\n" \
  "v_mfma_f32_16x16x32_bf16 %[d3], v[228:231], v[200:203], %[d3]\ns_waitcnt lgkmcnt(2)\nv_mfma_f32_16x16x32_bf16 %[d3], v[232:235], v[196:199], %[d3]\n" \
  "s_waitcnt lgkmcnt(0)\nv_mfma_f32_16x16x32_bf16 %[d3], v[236:239], v[192:195], %[d3]\ns_nop 7\n" \
  ""
#define CONV1D_ASM \
  "ds_read_b64 v[224:225], %[ga] offset:3776\nds_read_b64 v[226:227], %[ga] offset:3784\nds_read_b64 v[228:229], %[ga] offset:3264\n" \
  "ds_read_b64 v[230:231], %[ga] offset:3272\nds_read_b64 v[232:233], %[ga] offset:2752\nds_read_b64 v[234:235], %[ga] offset:2760\n" \
  "ds_read_b128 v[164:167], %[fa] offset:704\nds_read_b128 v[168:171], %[fa] offset:1216\nds_read_b128 v[180:183], %[fa] offset:2752\n" \
  "ds_read_b128 v[184:187], %[fa] offset:3264\nds_read_b128 v[160:163], %[fa] offset:192\nds_read_b128 v[172:175], %[fa] offset:1728\n" \
  "ds_read_b128 v[176:179], %[fa] offset:2240\ns_waitcnt lgkmcnt(6)\nv_mfma_f32_16x16x32_bf16 %[d0], v[224:227], v[164:167], 0\ns_waitcnt lgkmcnt(5)\n" \
  "v_mfma_f32_16x16x32_bf16 %[d1], v[224:227], v[168:171], 0\ns_waitcnt lgkmcnt(4)\nv_mfma_f32_16x16x32_bf16 %[d2], v[224:227], v[180:183], 0\n" \
  "s_mov_b32 m0, %[m0]\ns_nop 0\nglobal_load_lds_dwordx4 %[w0], %[wb0]\ns_waitcnt lgkmcnt(3)\nv_mfma_f32_16x16x32_bf16 %[d3], v[224:227], v[184:187], 0\n" \
  "ds_read_b64 v[236:237], %[ga] offset:2240\nds_read_b64 v[238:239], %[ga] offset:2248\ns_waitcnt lgkmcnt(4)\n" \
  "v_mfma_f32_16x16x32_bf16 %[d0], v[228:231], v[160:163], %[d0]\nv_mfma_f32_16x16x32_bf16 %[d1], v[228:231], v[164:167], %[d1]\ns_addk_i32 m0, 1024\n" \
  "s_nop 0\nglobal_load_lds_dwordx4 %[w1], %[wb0]\ns_waitcnt lgkmcnt(2)\nv_mfma_f32_16x16x32_bf16 %[d2], v[228:231], v[176:179], %[d2]\n" \
  "v_mfma_f32_16x16x32_bf16 %[d3], v[228:231], v[180:183], %[d3]\nds_read_b64 v[224:225], %[ga] offset:1728\nds_read_b64 v[226:227], %[ga] offset:1736\n" \
  "ds_read_b128 v[196:199], %[fa] offset:640\nv_mfma_f32_16x16x32_bf16 %[d1], v[232:235], v[160:163], %[d1]\ns_addk_i32 m0, 1024\ns_nop 0\n" \
  "global_load_lds_dwordx4 %[w2], %[wb0]\nv_mfma_f32_16x16x32_bf16 %[d2], v[232:235], v[172:175], %[d2]\n" \
  "v_mfma_f32_16x16x32_bf16 %[d3], v[232:235], v[176:179], %[d3]\nds_read_b64 v[228:229], %[ga] offset:1216\nds_read_b64 v[230:231], %[ga] offset:1224\n" \
  "ds_read_b128 v[200:203], %[fa] offset:1152\ns_waitcnt lgkmcnt(6)\nv_mfma_f32_16x16x32_bf16 %[d2], v[236:239], v[168:171], %[d2]\ns_addk_i32 m0, 1024\n" \
  "s_nop 0\nglobal_load_lds_dwordx4 %[w3], %[wb0]\nv_mfma_f32_16x16x32_bf16 %[d3], v[236:239], v[172:175], %[d3]\n" \
  "ds_read_b64 v[232:233], %[ga] offset:704\nds_read_b64 v[234:235], %[ga] offset:712\nds_read_b128 v[212:215], %[fa] offset:2688\ns_waitcnt lgkmcnt(7)\n" \
  "v_mfma_f32_16x16x32_bf16 %[d2], v[224:227], v[164:167], %[d2]\nv_mfma_f32_16x16x32_bf16 %[d3], v[224:227], v[168:171], %[d3]\ns_addk_i32 m0, 1024\n" \
  "s_nop 0\nglobal_load_lds_dwordx4 %[w4], %[wb0]\nds_read_b64 v[236:237], %[ga] offset:3712\nds_read_b64 v[238:239], %[ga] offset:3720\n" \
  "ds_read_b128 v[216:219], %[fa] offset:3200\ns_waitcnt lgkmcnt(7)\nv_mfma_f32_16x16x32_bf16 %[d2], v[228:231], v[160:163], %[d2]\n" \
  "v_mfma_f32_16x16x32_bf16 %[d3], v[228:231], v[164:167], %[d3]\nds_read_b64 v[224:225], %[ga] offset:3200\nds_read_b64 v[226:227], %[ga] offset:3208\n" \
  "ds_read_b128 v[192:195], %[fa] offset:128\ns_waitcnt lgkmcnt(7)\nv_mfma_f32_16x16x32_bf16 %[d3], v[232:235], v[160:163], %[d3]\ns_mov_b32 m0, %[m1]\n" \
  "s_nop 0\nglobal_load_lds_dwordx4 %[w0], %[wb1]\nds_read_b64 v[228:229], %[ga] offset:2688\nds_read_b64 v[230:231], %[ga] offset:2696\n" \
  "ds_read_b128 v[204:207], %[fa] offset:1664\nds_read_b128 v[208:211], %[fa] offset:2176\ns_waitcnt lgkmcnt(8)\n" \
  "v_mfma_f32_16x16x32_bf16 %[d0], v[236:239], v[196:199], %[d0]\nv_mfma_f32_16x16x32_bf16 %[d1], v[236:239], v[200:203], %[d1]\n" \
  "v_mfma_f32_16x16x32_bf16 %[d2], v[236:239], v[212:215], %[d2]\ns_addk_i32 m0, 1024\ns_nop 0\nglobal_load_lds_dwordx4 %[w1], %[wb1]\n" \
  "s_waitcnt lgkmcnt(7)\nv_mfma_f32_16x16x32_bf16 %[d3], v[236:239], v[216:219], %[d3]\nds_read_b64 v[232:233], %[ga] offset:2176\n" \
  "ds_read_b64 v[234:235], %[ga] offset:2184\ns_waitcnt lgkmcnt(6)\nv_mfma_f32_16x16x32_bf16 %[d0], v[224:227], v[192:195], %[d0]\n" \
  "v_mfma_f32_16x16x32_bf16 %[d1], v[224:227], v[196:199], %[d1]\ns_addk_i32 m0, 1024\ns_nop 0\nglobal_load_lds_dwordx4 %[w2], %[wb1]\n" \
  "s_waitcnt lgkmcnt(2)\nv_mfma_f32_16x16x32_bf16 %[d2], v[224:227], v[208:211], %[d2]\nv_mfma_f32_16x16x32_bf16 %[d3], v[224:227], v[212:215], %[d3]\n" \
  "ds_read_b64 v[236:237], %[ga] offset:1664\nds_read_b64 v[238:239], %[ga] offset:1672\nds_read_b128 v[164:167], %[fa] offset:576\n" \
  "v_mfma_f32_16x16x32_bf16 %[d1], v[228:231], v[192:195], %[d1]\ns_addk_i32 m0, 1024\ns_nop 0\nglobal_load_lds_dwordx4 %[w3], %[wb1]\n" \
  "v_mfma_f32_16x16x32_bf16 %[d2], v[228:231], v[204:207], %[d2]\nv_mfma_f32_16x16x32_bf16 %[d3], v[228:231], v[208:211], %[d3]\n" \
  "ds_read_b64 v[224:225], %[ga] offset:1152\nds_read_b64 v[226:227], %[ga] offset:1160\nds_read_b128 v[168:171], %[fa] offset:1088\n" \
  "s_waitcnt lgkmcnt(6)\nv_mfma_f32_16x16x32_bf16 %[d2], v[232:235], v[200:203], %[d2]\ns_addk_i32 m0, 1024\ns_nop 0\n" \
  "global_load_lds_dwordx4 %[w4], %[wb1]\nv_mfma_f32_16x16x32_bf16 %[d3], v[232:235], v[204:207], %[d3]\nds_read_b64 v[228:229], %[ga] offset:640\n" \
  "ds_read_b64 v[230:231], %[ga] offset:648\nds_read_b128 v[180:183], %[fa] offset:2624\ns_waitcnt lgkmcnt(7)\n" \
  "v_mfma_f32_16x16x32_bf16 %[d2], v[236:239], v[196:199], %[d2]\nv_mfma_f32_16x16x32_bf16 %[d3], v[236:239], v[200:203], %[d3]\ns_mov_b32 m0, %[m2]\n" \
  "s_nop 0\nglobal_load_lds_dwordx4 %[w0], %[wb2]\nds_read_b64 v[232:233], %[ga] offset:3648\nds_read_b64 v[234:235], %[ga] offset:3656\n" \
  "ds_read_b128 v[184:187], %[fa] offset:3136\ns_waitcnt lgkmcnt(7)\nv_mfma_f32_16x16x32_bf16 %[d2], v[224:227], v[192:195], %[d2]\n" \
  "v_mfma_f32_16x16x32_bf16 %[d3], v[224:227], v[196:199], %[d3]\nds_read_b64 v[236:237], %[ga] offset:3136\nds_read_b64 v[238:239], %[ga] offset:3144\n" \
  "ds_read_b128 v[160:163], %[fa] offset:64\ns_waitcnt lgkmcnt(7)\nv_mfma_f32_16x16x32_bf16 %[d3], v[228:231], v[192:195], %[d3]\ns_addk_i32 m0, 1024\n" \
  "s_nop 0\nglobal_load_lds_dwordx4 %[w1], %[wb2]\nds_read_b64 v[224:225], %[ga] offset:2624\nds_read_b64 v[226:227], %[ga] offset:2632\n" \
  "ds_read_b128 v[172:175], %[fa] offset:1600\nds_read_b128 v[176:179], %[fa] offset:2112\ns_waitcnt lgkmcnt(8)\n" \
  "v_mfma_f32_16x16x32_bf16 %[d0], v[232:235], v[164:167], %[d0]\nv_mfma_f32_16x16x32_bf16 %[d1], v[232:235], v[168:171], %[d1]\n" \
  "v_mfma_f32_16x16x32_bf16 %[d2], v[232:235], v[180:183], %[d2]\ns_addk_i32 m0, 1024\ns_nop 0\nglobal_load_lds_dwordx4 %[w2], %[wb2]\n" \
  "s_waitcnt lgkmcnt(7)\nv_mfma_f32_16x16x32_bf16 %[d3], v[232:235], v[184:187], %[d3]\nds_read_b64 v[228:229], %[ga] offset:2112\n" \
  "ds_read_b64 v[230:231], %[ga] offset:2120\ns_waitcnt lgkmcnt(6)\nv_mfma_f32_16x16x32_bf16 %[d0], v[236:239], v[160:163], %[d0]\n" \
  "v_mfma_f32_16x16x32_bf16 %[d1], v[236:239], v[164:167], %[d1]\ns_addk_i32 m0, 1024\ns_nop 0\nglobal_load_lds_dwordx4 %[w3], %[wb2]\n" \
  "s_waitcnt lgkmcnt(2)\nv_mfma_f32_16x16x32_bf16 %[d2], v[236:239], v[176:179], %[d2]\nv_mfma_f32_16x16x32_bf16 %[d3], v[236:239], v[180:183], %[d3]\n" \
  "ds_read_b64 v[232:233], %[ga] offset:1600\nds_read_b64 v[234:235], %[ga] offset:1608\nds_read_b128 v[196:199], %[fa] offset:512\n" \
  "v_mfma_f32_16x16x32_bf16 %[d1], v[224:227], v[160:163], %[d1]\ns_addk_i32 m0, 1024\ns_nop 0\nglobal_load_lds_dwordx4 %[w4], %[wb2]\n" \
  "v_mfma_f32_16x16x32_bf16 %[d2], v[224:227], v[172:175], %[d2]\nv_mfma_f32_16x16x32_bf16 %[d3], v[224:227], v[176:179], %[d3]\n" \
  "ds_read_b64 v[236:237], %[ga] offset:1088\nds_read_b64 v[238:239], %[ga] offset:1096\nds_read_b128 v[200:203], %[fa] offset:1024\n" \
  "s_waitcnt lgkmcnt(6)\nv_mfma_f32_16x16x32_bf16 %[d2], v[228:231], v[168:171], %[d2]\ns_mov_b32 m0, %[m3]\ns_nop 0\n" \
  "global_load_lds_dwordx4 %[w0], %[wb3]\nv_mfma_f32_16x16x32_bf16 %[d3], v[228:231], v[172:175], %[d3]\nds_read_b64 v[224:225], %[ga] offset:576\n" \
  "ds_read_b64 v[226:227], %[ga] offset:584\nds_read_b128 v[212:215], %[fa] offset:2560\ns_waitcnt lgkmcnt(7)\n" \
  "v_mfma_f32_16x16x32_bf16 %[d2], v[232:235], v[164:167], %[d2]\nv_mfma_f32_16x16x32_bf16 %[d3], v[232:235], v[168:171], %[d3]\ns_addk_i32 m0, 1024\n" \
  "s_nop 0\nglobal_load_lds_dwordx4 %[w1], %[wb3]\nds_read_b64 v[228:229], %[ga] offset:3584\nds_read_b64 v[230:231], %[ga] offset:3592\n" \
  "ds_read_b128 v[216:219], %[fa] offset:3072\ns_waitcnt lgkmcnt(7)\nv_mfma_f32_16x16x32_bf16 %[d2], v[236:239], v[160:163], %[d2]\n" \
  "v_mfma_f32_16x16x32_bf16 %[d3], v[236:239], v[164:167], %[d3]\nds_read_b64 v[232:233], %[ga] offset:3072\nds_read_b64 v[234:235], %[ga] offset:3080\n" \
  "ds_read_b128 v[192:195], %[fa] offset:0\ns_waitcnt lgkmcnt(7)\nv_mfma_f32_16x16x32_bf16 %[d3], v[224:227], v[160:163], %[d3]\ns_addk_i32 m0, 1024\n" \
  "s_nop 0\nglobal_load_lds_dwordx4 %[w2], %[wb3]\nds_read_b64 v[236:237], %[ga] offset:2560\nds_read_b64 v[238:239], %[ga] offset:2568\n" \
  "ds_read_b128 v[204:207], %[fa] offset:1536\nds_read_b128 v[208:211], %[fa] offset:2048\ns_waitcnt lgkmcnt(8)\n" \
  "v_mfma_f32_16x16x32_bf16 %[d0], v[228:231], v[196:199], %[d0]\nv_mfma_f32_16x16x32_bf16 %[d1], v[228:231], v[200:203], %[d1]\n" \
  "v_mfma_f32_16x16x32_bf16 %[d2], v[228:231], v[212:215], %[d2]\ns_addk_i32 m0, 1024\ns_nop 0\nglobal_load_lds_dwordx4 %[w3], %[wb3]\n" \
  "s_waitcnt lgkmcnt(7)\nv_mfma_f32_16x16x32_bf16 %[d3], v[228:231], v[216:219], %[d3]\nds_read_b64 v[224:225], %[ga] offset:2048\n" \
  "ds_read_b64 v[226:227], %[ga] offset:2056\ns_waitcnt lgkmcnt(6)\nv_mfma_f32_16x16x32_bf16 %[d0], v[232:235], v[192:195], %[d0]\n" \
  "v_mfma_f32_16x16x32_bf16 %[d1], v[232:235], v[196:199], %[d1]\ns_addk_i32 m0, 1024\ns_nop 0\nglobal_load_lds_dwordx4 %[w4], %[wb3]\n" \
  "s_waitcnt lgkmcnt(2)\nv_mfma_f32_16x16x32_bf16 %[d2], v[232:235], v[208:211], %[d2]\nv_mfma_f32_16x16x32_bf16 %[d3], v[232:235], v[212:215], %[d3]\n" \
  "ds_read_b64 v[228:229], %[ga] offset:1536\nds_read_b64 v[230:231], %[ga] offset:1544\nv_mfma_f32_16x16x32_bf16 %[d1], v[236:239], v[192:195], %[d1]\n" \
  "v_mfma_f32_16x16x32_bf16 %[d2], v[236:239], v[204:207], %[d2]\nv_mfma_f32_16x16x32_bf16 %[d3], v[236:239], v[208:211], %[d3]\n" \
  "ds_read_b64 v[232:233], %[ga] offset:1024\nds_read_b64 v[234:235], %[ga] offset:1032\ns_waitcnt lgkmcnt(4)\n" \
  "v_mfma_f32_16x16x32_bf16 %[d2], v[224:227], v[200:203], %[d2]\nv_mfma_f32_16x16x32_bf16 %[d3], v[224:227], v[204:207], %[d3]\n" \
  "ds_read_b64 v[236:237], %[ga] offset:512\nds_read_b64 v[238:239], %[ga] offset:520\ns_waitcnt lgkmcnt(4)\n" \
  "v_mfma_f32_16x16x32_bf16 %[d2], v[228:231], v[196:199], %[d2]\nv_mfma_f32_16x16x32_bf16 %[d3], v[228:231], v[200:203], %[d3]\ns_waitcnt lgkmcnt(2)\n" \
  "v_mfma_f32_16x16x32_bf16 %[d2], v[232:235], v[192:195], %[d2]\nv_mfma_f32_16x16x32_bf16 %[d3], v[232:235], v[196:199], %[d3]\ns_waitcnt lgkmcnt(0)\n" \
  "v_mfma_f32_16x16x32_bf16 %[d3], v[236:239], v[192:195], %[d3]\ns_nop 7\n" \
  ""
#define CHAIN0_ASM \
  "v_pk_mul_f32 v[148:149], v[138:139], v[132:133] op_sel_hi:[1,0]\nv_pk_mul_f32 v[150:151], v[130:131], v[132:133] op_sel_hi:[1,0]\n" \
  "v_cvt_pk_f16_f32 v16, v138, v130\nv_cvt_pk_f16_f32 v24, v139, v131\nv_pk_mul_f32 v[152:153], v[128:129], v[128:129] op_sel_hi:[1,0]\n" \
  "v_pk_mul_f32 v[154:155], v[136:137], v[134:135] op_sel_hi:[1,0]\n" \
  "v_pk_fma_f32 v[140:141], v[138:139], v[132:133], v[148:149] op_sel:[1,1,0] op_sel_hi:[0,1,1] neg_lo:[1,0,0]\n" \
  "v_pk_fma_f32 v[144:145], v[130:131], v[132:133], v[150:151] op_sel:[1,1,0] op_sel_hi:[0,1,1] neg_lo:[1,0,0]\n" \
  "v_pk_mul_f32 v[148:149], v[140:141], v[132:133] op_sel_hi:[1,0]\nv_pk_mul_f32 v[150:151], v[144:145], v[132:133] op_sel_hi:[1,0]\n" \
  "v_pk_fma_f32 v[156:157], v[128:129], v[128:129], v[152:153] op_sel:[1,1,0] op_sel_hi:[0,1,1] neg_lo:[1,0,0]\n" \
  "v_pk_fma_f32 v[174:175], v[136:137], v[134:135], v[154:155] op_sel:[1,1,0] op_sel_hi:[0,1,1] neg_lo:[1,0,0]\nv_cvt_pk_f16_f32 v17, v140, v144\n" \
  "v_cvt_pk_f16_f32 v25, v141, v145\nv_pk_fma_f32 v[142:143], v[140:141], v[132:133], v[148:149] op_sel:[1,1,0] op_sel_hi:[0,1,1] neg_lo:[1,0,0]\n" \
  "v_pk_fma_f32 v[146:147], v[144:145], v[132:133], v[150:151] op_sel:[1,1,0] op_sel_hi:[0,1,1] neg_lo:[1,0,0]\n" \
  "v_pk_mul_f32 v[152:153], v[156:157], v[156:157] op_sel_hi:[1,0]\nv_pk_mul_f32 v[154:155], v[174:175], v[134:135] op_sel_hi:[1,0]\n" \
  "v_pk_mul_f32 v[148:149], v[142:143], v[132:133] op_sel_hi:[1,0]\nv_pk_mul_f32 v[150:151], v[146:147], v[132:133] op_sel_hi:[1,0]\n" \
  "v_cvt_pk_f16_f32 v18, v142, v146\nv_cvt_pk_f16_f32 v26, v143, v147\n" \
  "v_pk_fma_f32 v[158:159], v[156:157], v[156:157], v[152:153] op_sel:[1,1,0] op_sel_hi:[0,1,1] neg_lo:[1,0,0]\n" \
  "v_pk_fma_f32 v[182:183], v[174:175], v[134:135], v[154:155] op_sel:[1,1,0] op_sel_hi:[0,1,1] neg_lo:[1,0,0]\n" \
  "v_pk_fma_f32 v[140:141], v[142:143], v[132:133], v[148:149] op_sel:[1,1,0] op_sel_hi:[0,1,1] neg_lo:[1,0,0]\n" \
  "v_pk_fma_f32 v[144:145], v[146:147], v[132:133], v[150:151] op_sel:[1,1,0] op_sel_hi:[0,1,1] neg_lo:[1,0,0]\n" \
  "v_pk_mul_f32 v[148:149], v[140:141], v[132:133] op_sel_hi:[1,0]\nv_pk_mul_f32 v[150:151], v[144:145], v[132:133] op_sel_hi:[1,0]\n" \
  "v_pk_mul_f32 v[152:153], v[136:137], v[128:129] op_sel_hi:[1,0]\nv_pk_mul_f32 v[154:155], v[182:183], v[134:135] op_sel_hi:[1,0]\n" \
  "v_cvt_pk_f16_f32 v19, v140, v144\nv_cvt_pk_f16_f32 v27, v141, v145\n" \
  "v_pk_fma_f32 v[142:143], v[140:141], v[132:133], v[148:149] op_sel:[1,1,0] op_sel_hi:[0,1,1] neg_lo:[1,0,0]\n" \
  "v_pk_fma_f32 v[146:147], v[144:145], v[132:133], v[150:151] op_sel:[1,1,0] op_sel_hi:[0,1,1] neg_lo:[1,0,0]\n" \
  "v_pk_fma_f32 v[160:161], v[136:137], v[128:129], v[152:153] op_sel:[1,1,0] op_sel_hi:[0,1,1] neg_lo:[1,0,0]\n" \
  "v_pk_fma_f32 v[186:187], v[182:183], v[134:135], v[154:155] op_sel:[1,1,0] op_sel_hi:[0,1,1] neg_lo:[1,0,0]\n" \
  "v_pk_mul_f32 v[148:149], v[142:143], v[132:133] op_sel_hi:[1,0]\nv_pk_mul_f32 v[150:151], v[146:147], v[132:133] op_sel_hi:[1,0]\n" \
  "v_cvt_pk_f16_f32 v20, v142, v146\nv_cvt_pk_f16_f32 v28, v143, v147\nv_pk_mul_f32 v[152:153], v[136:137], v[156:157] op_sel_hi:[1,0]\n" \
  "v_pk_mul_f32 v[154:155], v[186:187], v[134:135] op_sel_hi:[1,0]\n" \
  "v_pk_fma_f32 v[140:141], v[142:143], v[132:133], v[148:149] op_sel:[1,1,0] op_sel_hi:[0,1,1] neg_lo:[1,0,0]\n" \
  "v_pk_fma_f32 v[144:145], v[146:147], v[132:133], v[150:151] op_sel:[1,1,0] op_sel_hi:[0,1,1] neg_lo:[1,0,0]\n" \
  "v_pk_mul_f32 v[148:149], v[140:141], v[132:133] op_sel_hi:[1,0]\nv_pk_mul_f32 v[150:151], v[144:145], v[132:133] op_sel_hi:[1,0]\n" \
  "v_pk_fma_f32 v[162:163], v[136:137], v[156:157], v[152:153] op_sel:[1,1,0] op_sel_hi:[0,1,1] neg_lo:[1,0,0]\n" \
  "v_pk_fma_f32 v[190:191], v[186:187], v[134:135], v[154:155] op_sel:[1,1,0] op_sel_hi:[0,1,1] neg_lo:[1,0,0]\nv_cvt_pk_f16_f32 v21, v140, v144\n" \
  "v_cvt_pk_f16_f32 v29, v141, v145\nv_pk_fma_f32 v[142:143], v[140:141], v[132:133], v[148:149] op_sel:[1,1,0] op_sel_hi:[0,1,1] neg_lo:[1,0,0]\n" \
  "v_pk_fma_f32 v[146:147], v[144:145], v[132:133], v[150:151] op_sel:[1,1,0] op_sel_hi:[0,1,1] neg_lo:[1,0,0]\n" \
  "v_pk_mul_f32 v[152:153], v[136:137], v[158:159] op_sel_hi:[1,0]\nv_pk_mul_f32 v[154:155], v[190:191], v[134:135] op_sel_hi:[1,0]\n" \
  "v_pk_mul_f32 v[148:149], v[142:143], v[132:133] op_sel_hi:[1,0]\nv_pk_mul_f32 v[150:151], v[146:147], v[132:133] op_sel_hi:[1,0]\n" \
  "v_cvt_pk_f16_f32 v22, v142, v146\nv_cvt_pk_f16_f32 v30, v143, v147\n" \
  "v_pk_fma_f32 v[166:167], v[136:137], v[158:159], v[152:153] op_sel:[1,1,0] op_sel_hi:[0,1,1] neg_lo:[1,0,0]\n" \
  "v_pk_fma_f32 v[192:193], v[190:191], v[134:135], v[154:155] op_sel:[1,1,0] op_sel_hi:[0,1,1] neg_lo:[1,0,0]\n" \
  "v_pk_fma_f32 v[140:141], v[142:143], v[132:133], v[148:149] op_sel:[1,1,0] op_sel_hi:[0,1,1] neg_lo:[1,0,0]\n" \
  "v_pk_fma_f32 v[144:145], v[146:147], v[132:133], v[150:151] op_sel:[1,1,0] op_sel_hi:[0,1,1] neg_lo:[1,0,0]\n" \
  "v_pk_mul_f32 v[148:149], v[140:141], v[132:133] op_sel_hi:[1,0]\nv_pk_mul_f32 v[150:151], v[144:145], v[132:133] op_sel_hi:[1,0]\n" \
  "v_pk_mul_f32 v[152:153], v[162:163], v[128:129] op_sel_hi:[1,0]\nv_pk_mul_f32 v[154:155], v[192:193], v[134:135] op_sel_hi:[1,0]\n" \
  "v_cvt_pk_f16_f32 v23, v140, v144\nv_cvt_pk_f16_f32 v31, v141, v145\n" \
  "v_pk_fma_f32 v[142:143], v[140:141], v[132:133], v[148:149] op_sel:[1,1,0] op_sel_hi:[0,1,1] neg_lo:[1,0,0]\n" \
  "v_pk_fma_f32 v[146:147], v[144:145], v[132:133], v[150:151] op_sel:[1,1,0] op_sel_hi:[0,1,1] neg_lo:[1,0,0]\n" \
  "v_pk_fma_f32 v[164:165], v[162:163], v[128:129], v[152:153] op_sel:[1,1,0] op_sel_hi:[0,1,1] neg_lo:[1,0,0]\n" \
  "v_pk_fma_f32 v[194:195], v[192:193], v[134:135], v[154:155] op_sel:[1,1,0] op_sel_hi:[0,1,1] neg_lo:[1,0,0]\n" \
  "v_pk_mul_f32 v[148:149], v[142:143], v[132:133] op_sel_hi:[1,0]\nv_pk_mul_f32 v[150:151], v[146:147], v[132:133] op_sel_hi:[1,0]\n" \
  "v_cvt_pk_f16_f32 v32, v142, v146\nv_cvt_pk_f16_f32 v40, v143, v147\nv_pk_mul_f32 v[152:153], v[166:167], v[128:129] op_sel_hi:[1,0]\n" \
  "v_pk_mul_f32 v[154:155], v[194:195], v[134:135] op_sel_hi:[1,0]\n" \
  "v_pk_fma_f32 v[140:141], v[142:143], v[132:133], v[148:149] op_sel:[1,1,0] op_sel_hi:[0,1,1] neg_lo:[1,0,0]\n" \
  "v_pk_fma_f32 v[144:145], v[146:147], v[132:133], v[150:151] op_sel:[1,1,0] op_sel_hi:[0,1,1] neg_lo:[1,0,0]\n" \
  "v_pk_mul_f32 v[148:149], v[140:141], v[132:133] op_sel_hi:[1,0]\nv_pk_mul_f32 v[150:151], v[144:145], v[132:133] op_sel_hi:[1,0]\n" \
  "v_pk_fma_f32 v[168:169], v[166:167], v[128:129], v[152:153] op_sel:[1,1,0] op_sel_hi:[0,1,1] neg_lo:[1,0,0]\n" \
  "v_pk_fma_f32 v[196:197], v[194:195], v[134:135], v[154:155] op_sel:[1,1,0] op_sel_hi:[0,1,1] neg_lo:[1,0,0]\nv_cvt_pk_f16_f32 v33, v140, v144\n" \
  "v_cvt_pk_f16_f32 v41, v141, v145\nv_pk_fma_f32 v[142:143], v[140:141], v[132:133], v[148:149] op_sel:[1,1,0] op_sel_hi:[0,1,1] neg_lo:[1,0,0]\n" \
  "v_pk_fma_f32 v[146:147], v[144:145], v[132:133], v[150:151] op_sel:[1,1,0] op_sel_hi:[0,1,1] neg_lo:[1,0,0]\n" \
  "v_pk_mul_f32 v[152:153], v[166:167], v[156:157] op_sel_hi:[1,0]\nv_pk_mul_f32 v[154:155], v[196:197], v[134:135] op_sel_hi:[1,0]\n" \
  "v_pk_mul_f32 v[148:149], v[142:143], v[132:133] op_sel_hi:[1,0]\nv_pk_mul_f32 v[150:151], v[146:147], v[132:133] op_sel_hi:[1,0]\n" \
  "v_cvt_pk_f16_f32 v34, v142, v146\nv_cvt_pk_f16_f32 v42, v143, v147\n" \
  "v_pk_fma_f32 v[170:171], v[166:167], v[156:157], v[152:153] op_sel:[1,1,0] op_sel_hi:[0,1,1] neg_lo:[1,0,0]\n" \
  "v_pk_fma_f32 v[198:199], v[196:197], v[134:135], v[154:155] op_sel:[1,1,0] op_sel_hi:[0,1,1] neg_lo:[1,0,0]\n" \
  "v_pk_fma_f32 v[140:141], v[142:143], v[132:133], v[148:149] op_sel:[1,1,0] op_sel_hi:[0,1,1] neg_lo:[1,0,0]\n" \
  "v_pk_fma_f32 v[144:145], v[146:147], v[132:133], v[150:151] op_sel:[1,1,0] op_sel_hi:[0,1,1] neg_lo:[1,0,0]\n" \
  "v_pk_mul_f32 v[148:149], v[140:141], v[132:133] op_sel_hi:[1,0]\nv_pk_mul_f32 v[150:151], v[144:145], v[132:133] op_sel_hi:[1,0]\n" \
  "v_pk_mul_f32 v[152:153], v[174:175], v[156:157] op_sel_hi:[1,0]\nv_pk_mul_f32 v[154:155], v[174:175], v[158:159] op_sel_hi:[1,0]\n" \
  "v_cvt_pk_f16_f32 v35, v140, v144\nv_cvt_pk_f16_f32 v43, v141, v145\n" \
  "v_pk_fma_f32 v[142:143], v[140:141], v[132:133], v[148:149] op_sel:[1,1,0] op_sel_hi:[0,1,1] neg_lo:[1,0,0]\n" \
  "v_pk_fma_f32 v[146:147], v[144:145], v[132:133], v[150:151] op_sel:[1,1,0] op_sel_hi:[0,1,1] neg_lo:[1,0,0]\n" \
  "v_pk_fma_f32 v[176:177], v[174:175], v[156:157], v[152:153] op_sel:[1,1,0] op_sel_hi:[0,1,1] neg_lo:[1,0,0]\n" \
  "v_pk_fma_f32 v[178:179], v[174:175], v[158:159], v[154:155] op_sel:[1,1,0] op_sel_hi:[0,1,1] neg_lo:[1,0,0]\n" \
  "v_pk_mul_f32 v[148:149], v[142:143], v[132:133] op_sel_hi:[1,0]\nv_pk_mul_f32 v[150:151], v[146:147], v[132:133] op_sel_hi:[1,0]\n" \
  "v_cvt_pk_f16_f32 v36, v142, v146\nv_cvt_pk_f16_f32 v44, v143, v147\nv_pk_mul_f32 v[152:153], v[170:171], v[128:129] op_sel_hi:[1,0]\n" \
  "v_pk_mul_f32 v[154:155], v[182:183], v[158:159] op_sel_hi:[1,0]\n" \
  "v_pk_fma_f32 v[140:141], v[142:143], v[132:133], v[148:149] op_sel:[1,1,0] op_sel_hi:[0,1,1] neg_lo:[1,0,0]\n" \
  "v_pk_fma_f32 v[144:145], v[146:147], v[132:133], v[150:151] op_sel:[1,1,0] op_sel_hi:[0,1,1] neg_lo:[1,0,0]\n" \
  "v_pk_mul_f32 v[148:149], v[140:141], v[132:133] op_sel_hi:[1,0]\nv_pk_mul_f32 v[150:151], v[144:145], v[132:133] op_sel_hi:[1,0]\n" \
  "v_pk_fma_f32 v[172:173], v[170:171], v[128:129], v[152:153] op_sel:[1,1,0] op_sel_hi:[0,1,1] neg_lo:[1,0,0]\n" \
  "v_pk_fma_f32 v[184:185], v[182:183], v[158:159], v[154:155] op_sel:[1,1,0] op_sel_hi:[0,1,1] neg_lo:[1,0,0]\nv_cvt_pk_f16_f32 v37, v140, v144\n" \
  "v_cvt_pk_f16_f32 v45, v141, v145\nv_pk_fma_f32 v[142:143], v[140:141], v[132:133], v[148:149] op_sel:[1,1,0] op_sel_hi:[0,1,1] neg_lo:[1,0,0]\n" \
  "v_pk_fma_f32 v[146:147], v[144:145], v[132:133], v[150:151] op_sel:[1,1,0] op_sel_hi:[0,1,1] neg_lo:[1,0,0]\n" \
  "v_pk_mul_f32 v[152:153], v[178:179], v[156:157] op_sel_hi:[1,0]\nv_pk_mul_f32 v[154:155], v[186:187], v[158:159] op_sel_hi:[1,0]\n" \
  "v_pk_mul_f32 v[148:149], v[142:143], v[132:133] op_sel_hi:[1,0]\nv_pk_mul_f32 v[150:151], v[146:147], v[132:133] op_sel_hi:[1,0]\n" \
  "v_cvt_pk_f16_f32 v38, v142, v146\nv_cvt_pk_f16_f32 v46, v143, v147\n" \
  "v_pk_fma_f32 v[180:181], v[178:179], v[156:157], v[152:153] op_sel:[1,1,0] op_sel_hi:[0,1,1] neg_lo:[1,0,0]\n" \
  "v_pk_fma_f32 v[188:189], v[186:187], v[158:159], v[154:155] op_sel:[1,1,0] op_sel_hi:[0,1,1] neg_lo:[1,0,0]\n" \
  "v_pk_fma_f32 v[140:141], v[142:143], v[132:133], v[148:149] op_sel:[1,1,0] op_sel_hi:[0,1,1] neg_lo:[1,0,0]\n" \
  "v_pk_fma_f32 v[144:145], v[146:147], v[132:133], v[150:151] op_sel:[1,1,0] op_sel_hi:[0,1,1] neg_lo:[1,0,0]\nv_cvt_pk_f16_f32 v39, v140, v144\n" \
  "v_cvt_pk_f16_f32 v47, v141, v145\nv_cvt_pk_f16_f32 v48, v136, v160\nv_cvt_pk_f16_f32 v56, v137, v161\nv_cvt_pk_f16_f32 v49, v162, v164\n" \
  "v_cvt_pk_f16_f32 v57, v163, v165\nv_cvt_pk_f16_f32 v50, v166, v168\nv_cvt_pk_f16_f32 v58, v167, v169\nv_cvt_pk_f16_f32 v51, v170, v172\n" \
  "v_cvt_pk_f16_f32 v59, v171, v173\nv_cvt_pk_f16_f32 v52, v174, v176\nv_cvt_pk_f16_f32 v60, v175, v177\nv_cvt_pk_f16_f32 v53, v178, v180\n" \
  "v_cvt_pk_f16_f32 v61, v179, v181\nv_cvt_pk_f16_f32 v54, v182, v184\nv_cvt_pk_f16_f32 v62, v183, v185\nv_cvt_pk_f16_f32 v55, v186, v188\n" \
  "v_cvt_pk_f16_f32 v63, v187, v189\nv_cvt_pk_f16_f32 v64, v190, v192\nv_cvt_pk_f16_f32 v65, v194, v196\nv_cvt_pk_f16_f32 v66, v198, v198\n" \
  "v_mov_b32 v67, 0\nv_cvt_pk_f16_f32 v68, v191, v193\nv_cvt_pk_f16_f32 v69, v195, v197\nv_cvt_pk_f16_f32 v70, v199, v199\nv_mov_b32 v71, 0\n" \
  ""
#define CHAIN1_ASM \
  "v_pk_mul_f32 v[148:149], v[138:139], v[132:133] op_sel_hi:[1,0]\nv_pk_mul_f32 v[150:151], v[130:131], v[132:133] op_sel_hi:[1,0]\n" \
  "v_cvt_pk_f16_f32 v72, v138, v130\nv_cvt_pk_f16_f32 v80, v139, v131\nv_pk_mul_f32 v[152:153], v[128:129], v[128:129] op_sel_hi:[1,0]\n" \
  "v_pk_mul_f32 v[154:155], v[136:137], v[134:135] op_sel_hi:[1,0]\n" \
  "v_pk_fma_f32 v[140:141], v[138:139], v[132:133], v[148:149] op_sel:[1,1,0] op_sel_hi:[0,1,1] neg_lo:[1,0,0]\n" \
  "v_pk_fma_f32 v[144:145], v[130:131], v[132:133], v[150:151] op_sel:[1,1,0] op_sel_hi:[0,1,1] neg_lo:[1,0,0]\n" \
  "v_pk_mul_f32 v[148:149], v[140:141], v[132:133] op_sel_hi:[1,0]\nv_pk_mul_f32 v[150:151], v[144:145], v[132:133] op_sel_hi:[1,0]\n" \
  "v_pk_fma_f32 v[156:157], v[128:129], v[128:129], v[152:153] op_sel:[1,1,0] op_sel_hi:[0,1,1] neg_lo:[1,0,0]\n" \
  "v_pk_fma_f32 v[174:175], v[136:137], v[134:135], v[154:155] op_sel:[1,1,0] op_sel_hi:[0,1,1] neg_lo:[1,0,0]\nv_cvt_pk_f16_f32 v73, v140, v144\n" \
  "v_cvt_pk_f16_f32 v81, v141, v145\nv_pk_fma_f32 v[142:143], v[140:141], v[132:133], v[148:149] op_sel:[1,1,0] op_sel_hi:[0,1,1] neg_lo:[1,0,0]\n" \
  "v_pk_fma_f32 v[146:147], v[144:145], v[132:133], v[150:151] op_sel:[1,1,0] op_sel_hi:[0,1,1] neg_lo:[1,0,0]\n" \
  "v_pk_mul_f32 v[152:153], v[156:157], v[156:157] op_sel_hi:[1,0]\nv_pk_mul_f32 v[154:155], v[174:175], v[134:135] op_sel_hi:[1,0]\n" \
  "v_pk_mul_f32 v[148:149], v[142:143], v[132:133] op_sel_hi:[1,0]\nv_pk_mul_f32 v[150:151], v[146:147], v[132:133] op_sel_hi:[1,0]\n" \
  "v_cvt_pk_f16_f32 v74, v142, v146\nv_cvt_pk_f16_f32 v82, v143, v147\n" \
  "v_pk_fma_f32 v[158:159], v[156:157], v[156:157], v[152:153] op_sel:[1,1,0] op_sel_hi:[0,1,1] neg_lo:[1,0,0]\n" \
  "v_pk_fma_f32 v[182:183], v[174:175], v[134:135], v[154:155] op_sel:[1,1,0] op_sel_hi:[0,1,1] neg_lo:[1,0,0]\n" \
  "v_pk_fma_f32 v[140:141], v[142:143], v[132:133], v[148:149] op_sel:[1,1,0] op_sel_hi:[0,1,1] neg_lo:[1,0,0]\n" \
  "v_pk_fma_f32 v[144:145], v[146:147], v[132:133], v[150:151] op_sel:[1,1,0] op_sel_hi:[0,1,1] neg_lo:[1,0,0]\n" \
  "v_pk_mul_f32 v[148:149], v[140:141], v[132:133] op_sel_hi:[1,0]\nv_pk_mul_f32 v[150:151], v[144:145], v[132:133] op_sel_hi:[1,0]\n" \
  "v_pk_mul_f32 v[152:153], v[136:137], v[128:129] op_sel_hi:[1,0]\nv_pk_mul_f32 v[154:155], v[182:183], v[134:135] op_sel_hi:[1,0]\n" \
  "v_cvt_pk_f16_f32 v75, v140, v144\nv_cvt_pk_f16_f32 v83, v141, v145\n" \
  "v_pk_fma_f32 v[142:143], v[140:141], v[132:133], v[148:149] op_sel:[1,1,0] op_sel_hi:[0,1,1] neg_lo:[1,0,0]\n" \
  "v_pk_fma_f32 v[146:147], v[144:145], v[132:133], v[150:151] op_sel:[1,1,0] op_sel_hi:[0,1,1] neg_lo:[1,0,0]\n" \
  "v_pk_fma_f32 v[160:161], v[136:137], v[128:129], v[152:153] op_sel:[1,1,0] op_sel_hi:[0,1,1] neg_lo:[1,0,0]\n" \
  "v_pk_fma_f32 v[186:187], v[182:183], v[134:135], v[154:155] op_sel:[1,1,0] op_sel_hi:[0,1,1] neg_lo:[1,0,0]\n" \
  "v_pk_mul_f32 v[148:149], v[142:143], v[132:133] op_sel_hi:[1,0]\nv_pk_mul_f32 v[150:151], v[146:147], v[132:133] op_sel_hi:[1,0]\n" \
  "v_cvt_pk_f16_f32 v76, v142, v146\nv_cvt_pk_f16_f32 v84, v143, v147\nv_pk_mul_f32 v[152:153], v[136:137], v[156:157] op_sel_hi:[1,0]\n" \
  "v_pk_mul_f32 v[154:155], v[186:187], v[134:135] op_sel_hi:[1,0]\n" \
  "v_pk_fma_f32 v[140:141], v[142:143], v[132:133], v[148:149] op_sel:[1,1,0] op_sel_hi:[0,1,1] neg_lo:[1,0,0]\n" \
  "v_pk_fma_f32 v[144:145], v[146:147], v[132:133], v[150:151] op_sel:[1,1,0] op_sel_hi:[0,1,1] neg_lo:[1,0,0]\n" \
  "v_pk_mul_f32 v[148:149], v[140:141], v[132:133] op_sel_hi:[1,0]\nv_pk_mul_f32 v[150:151], v[144:145], v[132:133] op_sel_hi:[1,0]\n" \
  "v_pk_fma_f32 v[162:163], v[136:137], v[156:157], v[152:153] op_sel:[1,1,0] op_sel_hi:[0,1,1] neg_lo:[1,0,0]\n" \
  "v_pk_fma_f32 v[190:191], v[186:187], v[134:135], v[154:155] op_sel:[1,1,0] op_sel_hi:[0,1,1] neg_lo:[1,0,0]\nv_cvt_pk_f16_f32 v77, v140, v144\n" \
  "v_cvt_pk_f16_f32 v85, v141, v145\nv_pk_fma_f32 v[142:143], v[140:141], v[132:133], v[148:149] op_sel:[1,1,0] op_sel_hi:[0,1,1] neg_lo:[1,0,0]\n" \
  "v_pk_fma_f32 v[146:147], v[144:145], v[132:133], v[150:151] op_sel:[1,1,0] op_sel_hi:[0,1,1] neg_lo:[1,0,0]\n" \
  "v_pk_mul_f32 v[152:153], v[136:137], v[158:159] op_sel_hi:[1,0]\nv_pk_mul_f32 v[154:155], v[190:191], v[134:135] op_sel_hi:[1,0]\n" \
  "v_pk_mul_f32 v[148:149], v[142:143], v[132:133] op_sel_hi:[1,0]\nv_pk_mul_f32 v[150:151], v[146:147], v[132:133] op_sel_hi:[1,0]\n" \
  "v_cvt_pk_f16_f32 v78, v142, v146\nv_cvt_pk_f16_f32 v86, v143, v147\n" \
  "v_pk_fma_f32 v[166:167], v[136:137], v[158:159], v[152:153] op_sel:[1,1,0] op_sel_hi:[0,1,1] neg_lo:[1,0,0]\n" \
  "v_pk_fma_f32 v[192:193], v[190:191], v[134:135], v[154:155] op_sel:[1,1,0] op_sel_hi:[0,1,1] neg_lo:[1,0,0]\n" \
  "v_pk_fma_f32 v[140:141], v[142:143], v[132:133], v[148:149] op_sel:[1,1,0] op_sel_hi:[0,1,1] neg_lo:[1,0,0]\n" \
  "v_pk_fma_f32 v[144:145], v[146:147], v[132:133], v[150:151] op_sel:[1,1,0] op_sel_hi:[0,1,1] neg_lo:[1,0,0]\n" \
  "v_pk_mul_f32 v[148:149], v[140:141], v[132:133] op_sel_hi:[1,0]\nv_pk_mul_f32 v[150:151], v[144:145], v[132:133] op_sel_hi:[1,0]\n" \
  "v_pk_mul_f32 v[152:153], v[162:163], v[128:129] op_sel_hi:[1,0]\nv_pk_mul_f32 v[154:155], v[192:193], v[134:135] op_sel_hi:[1,0]\n" \
  "v_cvt_pk_f16_f32 v79, v140, v144\nv_cvt_pk_f16_f32 v87, v141, v145\n" \
  "v_pk_fma_f32 v[142:143], v[140:141], v[132:133], v[148:149] op_sel:[1,1,0] op_sel_hi:[0,1,1] neg_lo:[1,0,0]\n" \
  "v_pk_fma_f32 v[146:147], v[144:145], v[132:133], v[150:151] op_sel:[1,1,0] op_sel_hi:[0,1,1] neg_lo:[1,0,0]\n" \
  "v_pk_fma_f32 v[164:165], v[162:163], v[128:129], v[152:153] op_sel:[1,1,0] op_sel_hi:[0,1,1] neg_lo:[1,0,0]\n" \
  "v_pk_fma_f32 v[194:195], v[192:193], v[134:135], v[154:155] op_sel:[1,1,0] op_sel_hi:[0,1,1] neg_lo:[1,0,0]\n" \
  "v_pk_mul_f32 v[148:149], v[142:143], v[132:133] op_sel_hi:[1,0]\nv_pk_mul_f32 v[150:151], v[146:147], v[132:133] op_sel_hi:[1,0]\n" \
  "v_cvt_pk_f16_f32 v88, v142, v146\nv_cvt_pk_f16_f32 v96, v143, v147\nv_pk_mul_f32 v[152:153], v[166:167], v[128:129] op_sel_hi:[1,0]\n" \
  "v_pk_mul_f32 v[154:155], v[194:195], v[134:135] op_sel_hi:[1,0]\n" \
  "v_pk_fma_f32 v[140:141], v[142:143], v[132:133], v[148:149] op_sel:[1,1,0] op_sel_hi:[0,1,1] neg_lo:[1,0,0]\n" \
  "v_pk_fma_f32 v[144:145], v[146:147], v[132:133], v[150:151] op_sel:[1,1,0] op_sel_hi:[0,1,1] neg_lo:[1,0,0]\n" \
  "v_pk_mul_f32 v[148:149], v[140:141], v[132:133] op_sel_hi:[1,0]\nv_pk_mul_f32 v[150:151], v[144:145], v[132:133] op_sel_hi:[1,0]\n" \
  "v_pk_fma_f32 v[168:169], v[166:167], v[128:129], v[152:153] op_sel:[1,1,0] op_sel_hi:[0,1,1] neg_lo:[1,0,0]\n" \
  "v_pk_fma_f32 v[196:197], v[194:195], v[134:135], v[154:155] op_sel:[1,1,0] op_sel_hi:[0,1,1] neg_lo:[1,0,0]\nv_cvt_pk_f16_f32 v89, v140, v144\n" \
  "v_cvt_pk_f16_f32 v97, v141, v145\nv_pk_fma_f32 v[142:143], v[140:141], v[132:133], v[148:149] op_sel:[1,1,0] op_sel_hi:[0,1,1] neg_lo:[1,0,0]\n" \
  "v_pk_fma_f32 v[146:147], v[144:145], v[132:133], v[150:151] op_sel:[1,1,0] op_sel_hi:[0,1,1] neg_lo:[1,0,0]\n" \
  "v_pk_mul_f32 v[152:153], v[166:167], v[156:157] op_sel_hi:[1,0]\nv_pk_mul_f32 v[154:155], v[196:197], v[134:135] op_sel_hi:[1,0]\n" \
  "v_pk_mul_f32 v[148:149], v[142:143], v[132:133] op_sel_hi:[1,0]\nv_pk_mul_f32 v[150:151], v[146:147], v[132:133] op_sel_hi:[1,0]\n" \
  "v_cvt_pk_f16_f32 v90, v142, v146\nv_cvt_pk_f16_f32 v98, v143, v147\n" \
  "v_pk_fma_f32 v[170:171], v[166:167], v[156:157], v[152:153] op_sel:[1,1,0] op_sel_hi:[0,1,1] neg_lo:[1,0,0]\n" \
  "v_pk_fma_f32 v[198:199], v[196:197], v[134:135], v[154:155] op_sel:[1,1,0] op_sel_hi:[0,1,1] neg_lo:[1,0,0]\n" \
  "v_pk_fma_f32 v[140:141], v[142:143], v[132:133], v[148:149] op_sel:[1,1,0] op_sel_hi:[0,1,1] neg_lo:[1,0,0]\n" \
  "v_pk_fma_f32 v[144:145], v[146:147], v[132:133], v[150:151] op_sel:[1,1,0] op_sel_hi:[0,1,1] neg_lo:[1,0,0]\n" \
  "v_pk_mul_f32 v[148:149], v[140:141], v[132:133] op_sel_hi:[1,0]\nv_pk_mul_f32 v[150:151], v[144:145], v[132:133] op_sel_hi:[1,0]\n" \
  "v_pk_mul_f32 v[152:153], v[174:175], v[156:157] op_sel_hi:[1,0]\nv_pk_mul_f32 v[154:155], v[174:175], v[158:159] op_sel_hi:[1,0]\n" \
  "v_cvt_pk_f16_f32 v91, v140, v144\nv_cvt_pk_f16_f32 v99, v141, v145\n" \
  "v_pk_fma_f32 v[142:143], v[140:141], v[132:133], v[148:149] op_sel:[1,1,0] op_sel_hi:[0,1,1] neg_lo:[1,0,0]\n" \
  "v_pk_fma_f32 v[146:147], v[144:145], v[132:133], v[150:151] op_sel:[1,1,0] op_sel_hi:[0,1,1] neg_lo:[1,0,0]\n" \
  "v_pk_fma_f32 v[176:177], v[174:175], v[156:157], v[152:153] op_sel:[1,1,0] op_sel_hi:[0,1,1] neg_lo:[1,0,0]\n" \
  "v_pk_fma_f32 v[178:179], v[174:175], v[158:159], v[154:155] op_sel:[1,1,0] op_sel_hi:[0,1,1] neg_lo:[1,0,0]\n" \
  "v_pk_mul_f32 v[148:149], v[142:143], v[132:133] op_sel_hi:[1,0]\nv_pk_mul_f32 v[150:151], v[146:147], v[132:133] op_sel_hi:[1,0]\n" \
  "v_cvt_pk_f16_f32 v92, v142, v146\nv_cvt_pk_f16_f32 v100, v143, v147\nv_pk_mul_f32 v[152:153], v[170:171], v[128:129] op_sel_hi:[1,0]\n" \
  "v_pk_mul_f32 v[154:155], v[182:183], v[158:159] op_sel_hi:[1,0]\n" \
  "v_pk_fma_f32 v[140:141], v[142:143], v[132:133], v[148:149] op_sel:[1,1,0] op_sel_hi:[0,1,1] neg_lo:[1,0,0]\n" \
  "v_pk_fma_f32 v[144:145], v[146:147], v[132:133], v[150:151] op_sel:[1,1,0] op_sel_hi:[0,1,1] neg_lo:[1,0,0]\n" \
  "v_pk_mul_f32 v[148:149], v[140:141], v[132:133] op_sel_hi:[1,0]\nv_pk_mul_f32 v[150:151], v[144:145], v[132:133] op_sel_hi:[1,0]\n" \
  "v_pk_fma_f32 v[172:173], v[170:171], v[128:129], v[152:153] op_sel:[1,1,0] op_sel_hi:[0,1,1] neg_lo:[1,0,0]\n" \
  "v_pk_fma_f32 v[184:185], v[182:183], v[158:159], v[154:155] op_sel:[1,1,0] op_sel_hi:[0,1,1] neg_lo:[1,0,0]\nv_cvt_pk_f16_f32 v93, v140, v144\n" \
  "v_cvt_pk_f16_f32 v101, v141, v145\nv_pk_fma_f32 v[142:143], v[140:141], v[132:133], v[148:149] op_sel:[1,1,0] op_sel_hi:[0,1,1] neg_lo:[1,0,0]\n" \
  "v_pk_fma_f32 v[146:147], v[144:145], v[132:133], v[150:151] op_sel:[1,1,0] op_sel_hi:[0,1,1] neg_lo:[1,0,0]\n" \
  "v_pk_mul_f32 v[152:153], v[178:179], v[156:157] op_sel_hi:[1,0]\nv_pk_mul_f32 v[154:155], v[186:187], v[158:159] op_sel_hi:[1,0]\n" \
  "v_pk_mul_f32 v[148:149], v[142:143], v[132:133] op_sel_hi:[1,0]\nv_pk_mul_f32 v[150:151], v[146:147], v[132:133] op_sel_hi:[1,0]\n" \
  "v_cvt_pk_f16_f32 v94, v142, v146\nv_cvt_pk_f16_f32 v102, v143, v147\n" \
  "v_pk_fma_f32 v[180:181], v[178:179], v[156:157], v[152:153] op_sel:[1,1,0] op_sel_hi:[0,1,1] neg_lo:[1,0,0]\n" \
  "v_pk_fma_f32 v[188:189], v[186:187], v[158:159], v[154:155] op_sel:[1,1,0] op_sel_hi:[0,1,1] neg_lo:[1,0,0]\n" \
  "v_pk_fma_f32 v[140:141], v[142:143], v[132:133], v[148:149] op_sel:[1,1,0] op_sel_hi:[0,1,1] neg_lo:[1,0,0]\n" \
  "v_pk_fma_f32 v[144:145], v[146:147], v[132:133], v[150:151] op_sel:[1,1,0] op_sel_hi:[0,1,1] neg_lo:[1,0,0]\nv_cvt_pk_f16_f32 v95, v140, v144\n" \
  "v_cvt_pk_f16_f32 v103, v141, v145\nv_cvt_pk_f16_f32 v104, v136, v160\nv_cvt_pk_f16_f32 v112, v137, v161\nv_cvt_pk_f16_f32 v105, v162, v164\n" \
  "v_cvt_pk_f16_f32 v113, v163, v165\nv_cvt_pk_f16_f32 v106, v166, v168\nv_cvt_pk_f16_f32 v114, v167, v169\nv_cvt_pk_f16_f32 v107, v170, v172\n" \
  "v_cvt_pk_f16_f32 v115, v171, v173\nv_cvt_pk_f16_f32 v108, v174, v176\nv_cvt_pk_f16_f32 v116, v175, v177\nv_cvt_pk_f16_f32 v109, v178, v180\n" \
  "v_cvt_pk_f16_f32 v117, v179, v181\nv_cvt_pk_f16_f32 v110, v182, v184\nv_cvt_pk_f16_f32 v118, v183, v185\nv_cvt_pk_f16_f32 v111, v186, v188\n" \
  "v_cvt_pk_f16_f32 v119, v187, v189\nv_cvt_pk_f16_f32 v120, v190, v192\nv_cvt_pk_f16_f32 v121, v194, v196\nv_cvt_pk_f16_f32 v122, v198, v198\n" \
  "v_mov_b32 v123, 0\nv_cvt_pk_f16_f32 v124, v191, v193\nv_cvt_pk_f16_f32 v125, v195, v197\nv_cvt_pk_f16_f32 v126, v199, v199\nv_mov_b32 v127, 0\n" \
  ""
#define CHAIN_CLOBBERS "v140", "v141", "v142", "v143", "v144", "v145", "v146", "v147", "v148", "v149", "v150", "v151", "v152", "v153", "v154", "v155", "v156", "v157", "v158", "v159", "v160", "v161", "v162", "v163", "v164", "v165", "v166", "v167", "v168", "v169", "v170", "v171", "v172", "v173", "v174", "v175", "v176", "v177", "v178", "v179", "v180", "v181", "v182", "v183", "v184", "v185", "v186", "v187", "v188", "v189", "v190", "v191", "v192", "v193", "v194", "v195", "v196", "v197", "v198", "v199"
#define CHAIN0_OUT "=&{v[16:19]}"(PK[0][0]), "=&{v[20:23]}"(PK[0][1]), "=&{v[24:27]}"(PK[0][2]), "=&{v[28:31]}"(PK[0][3]), "=&{v[32:35]}"(PK[0][4]), "=&{v[36:39]}"(PK[0][5]), "=&{v[40:43]}"(PK[0][6]), "=&{v[44:47]}"(PK[0][7]), "=&{v[48:51]}"(PK[0][8]), "=&{v[52:55]}"(PK[0][9]), "=&{v[56:59]}"(PK[0][10]), "=&{v[60:63]}"(PK[0][11]), "=&{v[64:67]}"(PK[0][12]), "=&{v[68:71]}"(PK[0][13])
#define CHAIN1_OUT "=&{v[72:75]}"(PK[1][0]), "=&{v[76:79]}"(PK[1][1]), "=&{v[80:83]}"(PK[1][2]), "=&{v[84:87]}"(PK[1][3]), "=&{v[88:91]}"(PK[1][4]), "=&{v[92:95]}"(PK[1][5]), "=&{v[96:99]}"(PK[1][6]), "=&{v[100:103]}"(PK[1][7]), "=&{v[104:107]}"(PK[1][8]), "=&{v[108:111]}"(PK[1][9]), "=&{v[112:115]}"(PK[1][10]), "=&{v[116:119]}"(PK[1][11]), "=&{v[120:123]}"(PK[1][12]), "=&{v[124:127]}"(PK[1][13])
#define TMP_CLOBBERS "v208", "v209", "v210", "v211", "v212", "v213", "v214", "v215", "v216", "v217", "v218", "v219", "v220", "v221", "v222", "v223", "v224", "v225", "v226", "v227", "v228", "v229", "v230", "v231", "v232", "v233", "v234", "v235", "v236", "v237", "v238", "v239", "v240", "v241", "v242", "v243", "v244", "v245", "v246", "v247", "v248", "v249", "v250", "v251", "v252", "v253", "v254", "v255"
#define OL_OUT "=&{v[96:99]}"(ol[0]), "=&{v[100:103]}"(ol[1]), "=&{v[104:107]}"(ol[2]), "=&{v[108:111]}"(ol[3]), "=&{v[112:115]}"(ol[4]), "=&{v[116:119]}"(ol[5]), "=&{v[120:123]}"(ol[6]), "=&{v[124:127]}"(ol[7]), "=&{v[128:131]}"(ol[8]), "=&{v[132:135]}"(ol[9]), "=&{v[136:139]}"(ol[10]), "=&{v[140:143]}"(ol[11]), "=&{v[144:147]}"(ol[12]), "=&{v[148:151]}"(ol[13]), "=&{v[152:155]}"(ol[14]), "=&{v[156:159]}"(ol[15])
#define OL_IN "{v[96:99]}"(ol[0]), "{v[100:103]}"(ol[1]), "{v[104:107]}"(ol[2]), "{v[108:111]}"(ol[3]), "{v[112:115]}"(ol[4]), "{v[116:119]}"(ol[5]), "{v[120:123]}"(ol[6]), "{v[124:127]}"(ol[7]), "{v[128:131]}"(ol[8]), "{v[132:135]}"(ol[9]), "{v[136:139]}"(ol[10]), "{v[140:143]}"(ol[11]), "{v[144:147]}"(ol[12]), "{v[148:151]}"(ol[13]), "{v[152:155]}"(ol[14]), "{v[156:159]}"(ol[15])
#define OF_CLOBBERS "v96", "v97", "v98", "v99", "v100", "v101", "v102", "v103", "v104", "v105", "v106", "v107", "v108", "v109", "v110", "v111", "v112", "v113", "v114", "v115", "v116", "v117", "v118", "v119", "v120", "v121", "v122", "v123", "v124", "v125", "v126", "v127", "v128", "v129", "v130", "v131", "v132", "v133", "v134", "v135", "v136", "v137", "v138", "v139", "v140", "v141", "v142", "v143", "v144", "v145", "v146", "v147", "v148", "v149", "v150", "v151", "v152", "v153", "v154", "v155", "v156", "v157", "v158", "v159"
#define Q_CLOBBERS "v64", "v65", "v66", "v67", "v68", "v69", "v70", "v71", "v72", "v73", "v74", "v75", "v76", "v77", "v78", "v79", "v80", "v81", "v82", "v83", "v84", "v85", "v86", "v87", "v88", "v89", "v90", "v91", "v92", "v93", "v94", "v95", "v194", "v195", "v196", "v197", "v198", "v199", "v200", "v201", "v202", "v203", "v204", "v205", "v206", "v207", "v208", "v209", "v210", "v211", "v212", "v213", "v214", "v215", "v216", "v217", "v218", "v219", "v220", "v221", "v222", "v223", "v224", "v225", "v226", "v227", "v228", "v229", "v230", "v231", "v232", "v233", "v234", "v235", "v236", "v237", "v238", "v239", "v240", "v241", "v242", "v243", "v244", "v245", "v246", "v247", "v248", "v249", "v250", "v251", "v252", "v253", "v254", "v255"
#define KVW_OUT "=&{v[0:15]}"(wf[0]), "=&{v[16:31]}"(wf[1]), "=&{v[32:47]}"(wf[2]), "=&{v[48:63]}"(wf[3]), "=&{v[64:79]}"(wf[4])
#define KVW_IN "{v[0:15]}"(wf[0]), "{v[16:31]}"(wf[1]), "{v[32:47]}"(wf[2]), "{v[48:63]}"(wf[3]), "{v[64:79]}"(wf[4])
#define KVW_CLOBBERS "v0", "v1", "v2", "v3", "v4", "v5", "v6", "v7", "v8", "v9", "v10", "v11", "v12", "v13", "v14", "v15", "v16", "v17", "v18", "v19", "v20", "v21", "v22", "v23", "v24", "v25", "v26", "v27", "v28", "v29", "v30", "v31", "v32", "v33", "v34", "v35", "v36", "v37", "v38", "v39", "v40", "v41", "v42", "v43", "v44", "v45", "v46", "v47", "v48", "v49", "v50", "v51", "v52", "v53", "v54", "v55", "v56", "v57", "v58", "v59", "v60", "v61", "v62", "v63", "v64", "v65", "v66", "v67", "v68", "v69", "v70", "v71", "v72", "v73", "v74", "v75", "v76", "v77", "v78", "v79", "v208", "v209", "v210", "v211", "v212", "v213", "v214", "v215", "v216", "v217", "v218", "v219", "v220", "v221", "v222", "v223", "v224", "v225", "v226", "v227", "v228", "v229", "v230", "v231", "v232", "v233", "v234", "v235", "v236", "v237", "v238", "v239", "v240", "v241", "v242", "v243", "v244", "v245", "v246", "v247", "v248", "v249", "v250", "v251", "v252", "v253", "v254", "v255"
#define CONV_CLOBBERS "v160", "v161", "v162", "v163", "v164", "v165", "v166", "v167", "v168", "v169", "v170", "v171", "v172", "v173", "v174", "v175", "v176", "v177", "v178", "v179", "v180", "v181", "v182", "v183", "v184", "v185", "v186", "v187", "v188", "v189", "v190", "v191", "v192", "v193", "v194", "v195", "v196", "v197", "v198", "v199", "v200", "v201", "v202", "v203", "v204", "v205", "v206", "v207", "v208", "v209", "v210", "v211", "v212", "v213", "v214", "v215", "v216", "v217", "v218", "v219", "v220", "v221", "v222", "v223", "v224", "v225", "v226", "v227", "v228", "v229", "v230", "v231", "v232", "v233", "v234", "v235", "v236", "v237", "v238", "v239"

template <typename T> __device__ __forceinline__ T lrd(unsigned a) { return *(LAS T*)(size_t)a; }
template <typename T> __device__ __forceinline__ void lwr(unsigned a, T v) { *(LAS T*)(size_t)a = v; }
__device__ __forceinline__ s16x4 trd(unsigned a) { return __builtin_amdgcn_ds_read_tr16_b64_v4i16((LAS s16x4*)(size_t)a); }
__device__ __forceinline__ u32x4 cat(s16x4 a, s16x4 b) { return __builtin_bit_cast(u32x4, __builtin_shufflevector(a, b, 0, 1, 2, 3, 4, 5, 6, 7)); }
__device__ __forceinline__ bf16x8 asbf(u32x4 v) { return __builtin_bit_cast(bf16x8, v); }
__device__ __forceinline__ f16x8 ash(u32x4 v) { return __builtin_bit_cast(f16x8, v); }
__device__ __forceinline__ unsigned pkbf(float lo, float hi) {
  typedef __attribute__((ext_vector_type(2))) __bf16 bf16x2;
  bf16x2 r = {(__bf16)lo, (__bf16)hi};
  return __builtin_bit_cast(unsigned, r);
}
__device__ __forceinline__ unsigned pkh(float lo, float hi) { unsigned r; asm("v_cvt_pk_f16_f32 %0, %1, %2" : "=v"(r) : "v"(lo), "v"(hi)); return r; }
__device__ __forceinline__ float bflo(unsigned u) { return __uint_as_float(u << 16); }
__device__ __forceinline__ float bfhi(unsigned u) { return __uint_as_float(u & 0xffff0000u); }
__device__ __forceinline__ float rbf(float x) { return bflo(pkbf(x, x)); }
__device__ __forceinline__ f32x2 cmul(f32x2 p, f32x2 z) {
  f32x2 t, r;
  asm("v_pk_mul_f32 %0, %1, %2 op_sel_hi:[1,0]" : "=v"(t) : "v"(p), "v"(z));
  asm("v_pk_fma_f32 %0, %1, %2, %3 op_sel:[1,1,0] op_sel_hi:[0,1,1] neg_lo:[1,0,0]" : "=v"(r) : "v"(p), "v"(z), "v"(t));
  return r;
}
__device__ __forceinline__ f32x2 zpow(float al, float be, float p) {
  const float e = __builtin_amdgcn_exp2f(al * p), ph = __builtin_amdgcn_fractf(be * p);
  return f32x2{e * __builtin_amdgcn_cosf(ph), e * __builtin_amdgcn_sinf(ph)};
}
__device__ __forceinline__ void dma16(unsigned voff, u64 base, unsigned ldsa) {
  asm volatile("s_mov_b32 m0, %2\n\ts_nop 0\n\tglobal_load_lds_dwordx4 %0, %1" :: "v"(voff), "s"(base), "s"(ldsa) : "memory");
}
__device__ __forceinline__ void dma16c(unsigned voff, u64 base, unsigned ldsa) {
  asm volatile("s_mov_b32 m0, %2\n\ts_nop 0\n\tglobal_load_lds_dwordx4 %0, %1 sc1" :: "v"(voff), "s"(base), "s"(ldsa) : "memory");
}
#define VMWAIT(n) asm volatile("s_waitcnt vmcnt(" #n ")" ::: "memory")
template <unsigned K> __device__ __forceinline__ void sig(unsigned me) {
  unsigned t;
  asm volatile("v_mov_b32 %0, %2\n\tds_write_b32 %1, %0" : "=&v"(t) : "v"(me), "n"(K) : "memory");
}
__device__ __forceinline__ void sigv(unsigned me, unsigned k) { asm volatile("ds_write_b32 %0, %1" :: "v"(me), "v"(k) : "memory"); }
template <unsigned OFF> __device__ __forceinline__ void wait4(unsigned sy, unsigned k) {
  unsigned n = SPIN_MAX;
  asm volatile("1:\n\tds_read_b128 v[252:255], %1 offset:%3\n\ts_waitcnt lgkmcnt(0)\n\t"
               "v_min3_u32 v252, v252, v253, v254\n\tv_min_u32 v252, v252, v255\n\tv_cmp_gt_u32 vcc, %2, v252\n\ts_cbranch_vccz 2f\n\t"
               "s_sleep 1\n\ts_sub_u32 %0, %0, 1\n\ts_cbranch_scc0 1b\n2:"
               : "+s"(n) : "v"(sy), "s"(k), "n"(OFF) : "v252", "v253", "v254", "v255", "vcc", "scc", "memory");
}
__device__ __forceinline__ void wait1(unsigned a, unsigned k) {
  unsigned t, n = SPIN_MAX;
  asm volatile("1:\n\tds_read_b32 %0, %2\n\ts_waitcnt lgkmcnt(0)\n\tv_cmp_gt_u32 vcc, %3, %0\n\ts_cbranch_vccz 2f\n\t"
               "s_sleep 1\n\ts_sub_u32 %1, %1, 1\n\ts_cbranch_scc0 1b\n2:"
               : "=&v"(t), "+s"(n) : "v"(a), "s"(k) : "vcc", "scc", "memory");
}
#define SLOWSLEEP "4"
__device__ __forceinline__ void waitslow(unsigned a, unsigned k) {
  unsigned t, n = SPIN_MAX;
  asm volatile("1:\n\tds_read_b32 %0, %2\n\ts_waitcnt lgkmcnt(0)\n\tv_cmp_gt_u32 vcc, %3, %0\n\ts_cbranch_vccz 2f\n\t"
               "s_sleep " SLOWSLEEP "\n\ts_sub_u32 %1, %1, 1\n\ts_cbranch_scc0 1b\n2:"
               : "=&v"(t), "+s"(n) : "v"(a), "s"(k) : "vcc", "scc", "memory");
}
__device__ __forceinline__ u32x4 tag4(u64 tag) { return u32x4{(unsigned)tag, (unsigned)(tag >> 32), (unsigned)tag, (unsigned)(tag >> 32)}; }
__device__ __forceinline__ void flag_set(char* ws, unsigned off_lane, u32x4 tv) {
  asm volatile("s_mov_b64 exec, 0xff\n\tglobal_store_dwordx4 %0, %1, %2 sc1\n\ts_mov_b64 exec, -1" :: "v"(off_lane), "v"(tv), "s"(ws) : "memory");
}
__device__ __forceinline__ void poll1(const char* ws, unsigned off, u64 tag) {
  u64 t; unsigned n = SPIN_MAX;
  asm volatile("1:\n\tglobal_load_dwordx2 %0, %2, %3 sc1\n\ts_waitcnt vmcnt(0)\n\tv_cmp_ne_u64 vcc, %4, %0\n\ts_and_b64 exec, exec, vcc\n\ts_cbranch_execz 2f\n\t"
               "s_sleep 1\n\ts_sub_u32 %1, %1, 1\n\ts_cbranch_scc0 1b\n2:\n\ts_mov_b64 exec, -1"
               : "=&v"(t), "+s"(n) : "v"(off), "s"(ws), "s"(tag) : "vcc", "scc", "memory");
}
#define MFMA32(a, b, c) __builtin_amdgcn_mfma_f32_32x32x16_bf16(a, b, c, 0, 0, 0)
#define SC1 16
#define CSTAMP(i) do {} while (0)
#define STAMP(i) do {} while (0)
#define STAMP_DECL
#define ST_OP
#define STAMP_OUT() do {} while (0)

__global__ void __launch_bounds__(512, 2)
gated_mix_kernel(const u16* __restrict__ xg, const u16* __restrict__ lar, const u16* __restrict__ aim, const u16* __restrict__ crg,
                 const u16* __restrict__ cig, const u16* __restrict__ ldt, const u16* __restrict__ dsk, const u16* __restrict__ wq,
                 const u16* __restrict__ wk, const u16* __restrict__ wv, const u16* __restrict__ wo, const u16* __restrict__ gw,
                 u16* __restrict__ out, char* ws) {
  const int tid = threadIdx.x;
  const int w = __builtin_amdgcn_readfirstlane(tid >> 6);
  const int b = blockIdx.x;
  const int r = 4 * (b & 7) + ((b >> 3) & 3), g = b >> 5, id = r * 8 + g;
#define LANE_IDS()                                                                                                        \
  int lane;                                                                                                               \
  asm volatile("v_mbcnt_lo_u32_b32 %0, -1, 0\n\tv_mbcnt_hi_u32_b32 %0, -1, %0" : "=v"(lane));                            \
  __builtin_assume(lane >= 0 && lane < 64);                                                                               \
  const int g4 = lane >> 4, q4 = (lane >> 2) & 3, p4 = lane & 3, c16 = lane & 15, h = lane >> 5, m32 = lane & 31;         \
  (void)g4; (void)q4; (void)p4; (void)c16; (void)h; (void)m32
  const __amdgpu_buffer_rsrc_t wsr = __builtin_amdgcn_make_buffer_rsrc(ws, 0, WS_END, 0x00020000);
  u64 pc; unsigned len;
  asm volatile("s_getpc_b64 %0\n.Lgm_here:\n\ts_mov_b32 %1, .Lgm_end_main-.Lgm_here\n\ts_max_i32 %1, %1, .Lgm_end_aux-.Lgm_here" : "=s"(pc), "=s"(len) :: "scc");
  if (tid < 16) lwr<unsigned>(L_SYNC + 4 * tid, 0u);
  asm volatile("s_waitcnt lgkmcnt(0)\n\ts_barrier"
               :: "s"(xg), "s"(lar), "s"(aim), "s"(crg), "s"(cig), "s"(ldt), "s"(dsk), "s"(wq), "s"(wk), "s"(wv), "s"(gw), "s"(ws), "s"(wo), "s"(out) : "memory");
  u64 tag;
#define TAG_LOAD() asm volatile("s_load_dwordx2 %0, %1, 0x0" : "=s"(tag) : "s"(ws) : "memory")
#define TAG_WAIT() asm volatile("s_waitcnt lgkmcnt(0)" : "+s"(tag) :: "memory"); tag += 0x9E3779B97F4A7C15ull

  if (w < 4) {
    LANE_IDS();
    STAMP_DECL;
    STAMP(0); CSTAMP(28);
    unsigned sy = L_SYNC, me = L_SYNC + 4 * w;
    PIN(sy); PIN(me);
    unsigned gv[3];
    const int par = w & 1, kh = w >> 1;
    {
      const int co = g < 2 ? 8 * g : (g == 2 ? 12 : min(4 * (g - 3), 12));
      const u64 kb = (u64)(g < 3 ? wk : wv) + co * 2;
      dma16(lane * 40, kb + w * 2560, L_KVW + w * 1024);
      dma16(lane * 40, kb + (w + 4) * 2560, L_KVW + (w + 4) * 1024);
      dma16(lane * 40, kb + (par + 8) * 2560, L_KVW + (par + 8) * 1024);
#pragma unroll
      for (int i = 0; i < 3; i++)
        asm volatile("global_load_ushort %0, %1, %2" : "=v"(gv[i]) : "v"(min(64 * w + lane + 256 * i, 639) * 2), "s"(gw) : "memory");
    }
    const u64 xb = (u64)xg + (u64)r * 81920 + w * 20480;
#define XJ(j) ((j) + g >= 10 ? (j) + g - 10 : ((j) + g >= 5 ? (j) + g - 5 : (j) + g))
#define XD(j0) do { const int j = XJ(j0); const int n_ = 64 * (j) + lane, row_ = n_ / 80, c_ = n_ - row_ * 80; unsigned t_;                                       \
      asm volatile(XDMA_ASM : [t] "=&v"(t_) : [v] "v"(row_ * 1280 + ((c_ ^ row_) << 4)), [g0] "s"(xb), [g1] "s"(xb + 5120),                \
                   [g2] "s"(xb + 10240), [g3] "s"(xb + 15360), [m] "s"(L_X + w * 20480 + (j) * 1024) : "scc", "memory"); } while (0)
    __builtin_amdgcn_s_setprio(2);
    XD(0); XD(1); XD(2);
    TAG_LOAD();
    unsigned xw[4], kwa = (p4 < 2 ? L_KVW + p4 * 8 : L_G16) + (8 * g4 + q4) * 16;
#pragma unroll
    for (int i = 0; i < 4; i++) { xw[i] = L_X + (16 * w + c16) * 1280 + (((4 * i + g4) ^ c16) << 4); PIN(xw[i]); }
    unsigned kvo = L_KVO + (g < 2 ? (16 * w + c16) * 16 + g4 * 8 : (16 * w + c16) * (g == 2 ? 8 : 2));
    const bool kvon = g < 2 ? g4 < 2 : g4 == ((g == 2 || g == 7) ? 1 : 0);
    unsigned gd[3];
#pragma unroll
    for (int i = 0; i < 3; i++) { gd[i] = L_G16 + min(64 * w + lane + 256 * i, 639) * 16; PIN(gd[i]); }
    PIN(kwa); PIN(kvo);
    asm volatile("s_waitcnt vmcnt(12)\n\tds_write_b16 %0, %3\n\tds_write_b16 %1, %4\n\tds_write_b16 %2, %5"
                 :: "v"(gd[0]), "v"(gd[1]), "v"(gd[2]), "v"(gv[0]), "v"(gv[1]), "v"(gv[2]) : "memory");
    sig<1>(me);
    XD(3); XD(4);
    STAMP(22);
    wait4<0>(sy, 1);
    STAMP(1);
    f32x16 wf[5];
    asm volatile(KVW_ASM : KVW_OUT : [wa] "v"(kwa) : "memory");
    unsigned qwa = L_RING + (8 * h + 2 * q4) * 160 + 40 * w + 8 * ((g4 & 1) ? 4 : 2 * (p4 & 1) + (p4 >> 1));
    unsigned xq = L_X + m32 * 1280 + ((h ^ c16) << 4);
    PIN(qwa); PIN(xq);
    TAG_WAIT();
    constexpr float QS = 0.22360679774997896f * 1.4426950408889634f;
    float gt;
    {
      f32x4 d;
      asm volatile(KVPROJ_ASM : [d] "=&v"(d) : [x0] "v"(xw[0]), [x1] "v"(xw[1]), [x2] "v"(xw[2]), [x3] "v"(xw[3]), KVW_IN
                   : TMP_CLOBBERS, "memory");
      if (g < 3) {
        if (kvon) lwr<u32x2>(kvo, u32x2{pkbf(d[0] * QS, d[1] * QS), pkbf(d[2] * QS, d[3] * QS)});
      } else if (kvon) {
        const unsigned o0 = pkbf(d[0], d[1]), o1 = pkbf(d[2], d[3]);
        lwr<u16>(kvo, (u16)o0); lwr<u16>(kvo + 128, (u16)(o0 >> 16)); lwr<u16>(kvo + 256, (u16)o1); lwr<u16>(kvo + 384, (u16)(o1 >> 16));
      }
      sig<3>(me);
      gt = d[0];
    }
    STAMP(2);
    const unsigned LB = L_RING + w * SLOT;
    unsigned go[5], qx, qy, qz, spa, kA, kB0, kB1, vA[4], ob8, ob4, ex, ey;
    f32x16 S3, O0, O1, O2, O3;
    u32x4 v0, v1;
    STAMP(24);
    const u64 kvb = (u64)ws + WS_KV;
    unsigned ko = (((4 * (b & 7) + 4) & 31) + kh) * 5120, late;
    u32x4 qa[4]; u32x2 qb[4];
    {
      unsigned n = SPIN_MAX;
      wait4<0>(sy, 3);
      STAMP(21);
      const unsigned ot = WS_O + 4 * r * 20480;
      asm volatile(QLOOP_ASM
                   : [n] "+s"(n), [late] "=&s"(late), [qx] "=&v"(qx), [qy] "=&v"(qy), [qz] "=&v"(qz),
                     [spa] "=&v"(spa), "=&{v166}"(kA), "=&{v167}"(kB0), "=&{v168}"(kB1), "=&{v169}"(vA[0]), "=&{v170}"(vA[1]), "=&{v171}"(vA[2]),
                     "=&{v172}"(vA[3]), "=&{v173}"(go[0]), "=&{v174}"(go[2]), "=&{v175}"(go[3]), "=&{v176}"(go[4]), "=&{v177}"(ex),
                     "=&{v179}"(ey), "=&{v180}"(ob8), "=&{v181}"(ob4),
                     "=&{v[0:15]}"(O0), "=&{v[16:31]}"(O1), "=&{v[32:47]}"(O2), "=&{v[48:63]}"(O3), "=&{v[112:127]}"(S3), "=&{v[158:161]}"(v0),
                     "=&{v[162:165]}"(v1), "=&{v[128:131]}"(qa[0]), "=&{v[132:135]}"(qa[1]), "=&{v[136:139]}"(qa[2]), "=&{v[140:143]}"(qa[3]),
                     "=&{v[144:145]}"(qb[0]), "=&{v[146:147]}"(qb[1]), "=&{v[148:149]}"(qb[2]), "=&{v[150:151]}"(qb[3])
                   : [xb] "v"(xq), [wa] "v"(qwa), [mp] "v"(sy), [me] "v"(me), [ln] "v"(lane), [lb] "s"(LB), [kb] "s"(kvb + ko), [c2560] "s"(2560),
                     [lbz] "s"(LB + 5136), [lbv] "s"(LB + 2560), [lbo] "s"(LB + 5120), [sqx] "s"(L_QX + 2560 * w), [sqy] "s"(L_QX + 2560 * (w ^ 2)),
                     [sqz] "s"(L_QX + 2560 * (w ^ 2) + 2048), [sey] "s"(L_RING + (w ^ 2) * SLOT), [so8] "s"(ot + (8 * g + 2 * w) * 256),
                     [so4] "s"(ot + 16384 + (4 * g + w) * 128)
                   : Q_CLOBBERS, "s28", "s29", "s36", "s37", "s38", "s39", "s40", "s41", "vcc", "scc", "memory");
    }
    STAMP(3);
    f32x16 S0, S1, S2;
    u64 okm;
#define KV_FETCH(base, lds)                                                                                                          \
    asm volatile("s_mov_b32 m0, %5\n\ts_nop 0\n\tglobal_load_lds_dwordx4 %0, %4 sc1\n\tglobal_load_lds_dwordx4 %0, %4 offset:1024 sc1\n\t"    \
                 "s_add_u32 m0, %5, 2048\n\ts_nop 0\n\tglobal_load_lds_dwordx4 %1, %4 sc1\n\ts_add_u32 m0, %5, 3072\n\ts_nop 0\n\t"          \
                 "global_load_lds_dwordx4 %2, %4 sc1\n\ts_add_u32 m0, %5, 4096\n\ts_nop 0\n\tglobal_load_lds_dwordx4 %3, %4 sc1"              \
                 :: "v"(go[0]), "v"(go[2]), "v"(go[3]), "v"(go[4]), "s"(base), "s"(lds) : "scc", "memory")
    if (late) {
      wait1(sy + 32, 11);
      KV_FETCH(kvb + ko, LB);
    }
    KV_FETCH(kvb + ko + 10240, LB + SLOTS);
    if (late) STAMP(25);
    STAMP(5); CSTAMP(29);
    {
      ko += 20480;
      ko -= ko >= 163840 ? 163840 : 0;
      u64 gbase = kvb + ko;
      unsigned it = 7, n = SPIN_MAX, tg = 12;
      const unsigned idxv = 0xdddd;
      u32x4 ka; u32x2 kb;
      asm volatile(ATTN_LOOP_ASM
        : "+{v[0:15]}"(O0), "+{v[16:31]}"(O1), "+{v[32:47]}"(O2), "+{v[48:63]}"(O3),
          "=&{v[64:79]}"(S0), "=&{v[80:95]}"(S1), "=&{v[96:111]}"(S2), "+{v[112:127]}"(S3),
          "=&{v[152:155]}"(ka), "=&{v[156:157]}"(kb), "+{v[158:161]}"(v0), "+{v[162:165]}"(v1),
          "+{s[22:23]}"(gbase), [it] "+s"(it), [o] "+s"(ko), [n] "+s"(n), [tg] "+s"(tg), [ok] "=&s"(okm), "+{v180}"(ob8), "+{v181}"(ob4) ST_OP
        : "{v[128:131]}"(qa[0]), "{v[132:135]}"(qa[1]), "{v[136:139]}"(qa[2]), "{v[140:143]}"(qa[3]),
          "{v[144:145]}"(qb[0]), "{v[146:147]}"(qb[1]), "{v[148:149]}"(qb[2]), "{v[150:151]}"(qb[3]),
          "{v166}"(kA), "{v167}"(kB0), "{v168}"(kB1), "{v169}"(vA[0]), "{v170}"(vA[1]), "{v171}"(vA[2]), "{v172}"(vA[3]),
          "{v173}"(go[0]), "{v174}"(go[2]), "{v175}"(go[3]), "{v176}"(go[4]), "{v178}"(idxv), [lb] "s"(LB),
          [kl] "s"((unsigned)kvb), [kh] "s"((unsigned)(kvb >> 32)), "{v177}"(ex), "{v179}"(ey), [me] "v"(me), [pa] "v"(me ^ 8), [sy] "v"(sy),
          [ws] "s"(ws), [lo] "s"(1e-24f)
        : "v182", "s24", "s25", "vcc", "scc", "memory");
    }
    STAMP(6); CSTAMP(30);
    __builtin_amdgcn_s_setprio(0);
    {
      LANE_IDS();
      if (__builtin_expect(okm != ~0ull, 0)) {
        float qf[20], oa[20];
#pragma unroll
        for (int i = 0; i < 4; i++) {
          const auto sw = __builtin_amdgcn_permlane32_swap(qa[2][i], qa[3][i], false, false);
          qf[2 * i] = bflo(sw[0]); qf[2 * i + 1] = bfhi(sw[0]); qf[8 + 2 * i] = bflo(sw[1]); qf[9 + 2 * i] = bfhi(sw[1]);
        }
#pragma unroll
        for (int i = 0; i < 2; i++) {
          const auto sw = __builtin_amdgcn_permlane32_swap(qb[2][i], qb[3][i], false, false);
          qf[16 + 2 * i] = bflo(sw[0]); qf[17 + 2 * i] = bfhi(sw[0]);
        }
#pragma unroll
        for (int i = 0; i < 20; i++) oa[i] = 0.f;
        float mx = -INFINITY, l = 0.f;
#pragma unroll 1
        for (int key = 0; key < 2048; key++) {
          const unsigned kb0 = WS_KV + (key >> 6) * 5120, kk = key & 63;
          const u32x4 k0 = __builtin_amdgcn_raw_buffer_load_b128(wsr, kb0 + kk * 16, 0, SC1);
          const u32x4 k1 = __builtin_amdgcn_raw_buffer_load_b128(wsr, kb0 + 1024 + kk * 16, 0, SC1);
          const u32x2 k2 = __builtin_amdgcn_raw_buffer_load_b64(wsr, kb0 + 2048 + kk * 8, 0, SC1);
          float s = 0.f;
#pragma unroll
          for (int i = 0; i < 4; i++)
            s += qf[2 * i] * bflo(k0[i]) + qf[2 * i + 1] * bfhi(k0[i]) + qf[8 + 2 * i] * bflo(k1[i]) + qf[9 + 2 * i] * bfhi(k1[i]);
#pragma unroll
          for (int i = 0; i < 2; i++) s += qf[16 + 2 * i] * bflo(k2[i]) + qf[17 + 2 * i] * bfhi(k2[i]);
          const float mn = fmaxf(mx, s), al = __builtin_amdgcn_exp2f(mx - mn), pr = __builtin_amdgcn_exp2f(s - mn);
          mx = mn; l = l * al + pr;
#pragma unroll
          for (int d = 0; d < 20; d++)
            oa[d] = oa[d] * al + pr * bflo((u16)__builtin_amdgcn_raw_buffer_load_b16(wsr, kb0 + 2560 + d * 128 + kk * 2, 0, SC1));
        }
        const float inv = 1.f / l;
        const unsigned tb = WS_O + (4 * r + g4) * 20480;
#pragma unroll
        for (int i = 0; i < 5; i++) {
          const u32x2 o4 = {pkbf(oa[4 * i] * inv, oa[4 * i + 1] * inv), pkbf(oa[4 * i + 2] * inv, oa[4 * i + 3] * inv)};
          const unsigned off = i < 4 ? (8 * g + 2 * w + (i >> 1)) * 256 + c16 * 16 + (i & 1) * 8 : 16384 + (4 * g + w) * 128 + c16 * 8;
          __builtin_amdgcn_raw_buffer_store_b64(o4, wsr, tb + off, 0, SC1);
        }
      }
    }
    {
      LANE_IDS();
      unsigned a0 = (8 * g4 + q4 + 4 * (g4 & 1)) * 160 + 8 * p4, a1 = (8 * g4 + q4 + 4 * (1 - (g4 & 1))) * 160 + 8 * p4;
      unsigned b0 = a0 + 61440, b1 = a1 + 61440, c0 = a0 + L_WOD, c1 = a1 + L_WOD;
      unsigned ata = L_AT + (4 * g4 + q4) * 160 + (16 * w + 4 * p4) * 2, fo = WS_F4 + (r * 32 + m32) * 128, lo = lane * 16, l2 = g4 * 256 + c16 * 8;
      unsigned oo = ((64 * r + 16 * w + c16) * 640 + 80 * g + 4 * g4) * 2;
      PIN(a0); PIN(a1); PIN(b0); PIN(b1); PIN(c0); PIN(c1); PIN(ata); PIN(fo); PIN(lo); PIN(l2); PIN(oo);
      const auto s1 = __builtin_amdgcn_permlane32_swap(__float_as_uint(gt), __float_as_uint(gt), false, false);
      const auto s2 = __builtin_amdgcn_permlane16_swap(s1[1], s1[1], false, false);
      gt = rbf(__builtin_amdgcn_rcpf(1.f + __builtin_amdgcn_exp2f(-1.4426950408889634f * rbf(__uint_as_float(s2[0])))));
      unsigned f4s = WS_F4 + (id * 4 + w) * 128 + lane * 16;
      u32x4 tv = tag4(tag);
      PIN(gt); PIN(f4s); PIN(tv);
      VMWAIT(0);
      flag_set(ws, f4s, tv);
      STAMP(7);
      wait4<16>(sy, 4);
      __builtin_amdgcn_s_sleep(F4DELAY);
      STAMP(8);
      f32x4 acc[5];
      u32x4 o16, o17, o18, o19;
      const u64 ob = (u64)ws + WS_O + (4 * r + w) * 20480;
#pragma unroll 1
      for (int pass = 0; pass < 2; pass++) {
        unsigned mx;
        u32x4 ol[16];
        asm volatile(OLOAD_ASM
                     : OL_OUT, "=&{v[160:163]}"(o16), "=&{v[164:167]}"(o17), "=&{v[168:171]}"(o18), "=&{v[172:175]}"(o19)
                     : [lo] "v"(lo), [l2] "v"(l2), [ob0] "s"(ob), [ob1] "s"(ob + 4096), [ob2] "s"(ob + 8192), [ob3] "s"(ob + 12288), [ob4] "s"(ob + 16384)
                     : "memory");
        wait4<16>(sy, 9);
        asm volatile(OPROJ_A_ASM
                     : [d0] "=&v"(acc[0]), [d1] "=&v"(acc[1]), [d2] "=&v"(acc[2]), [d3] "=&v"(acc[3]), [d4] "=&v"(acc[4]), [mx] "=&v"(mx),
                       "+{v[160:163]}"(o16), "+{v[164:167]}"(o17), "+{v[168:171]}"(o18), "+{v[172:175]}"(o19)
                     : [a0] "v"(a0), [a1] "v"(a1), [b0] "v"(b0), [b1] "v"(b1), OL_IN
                     : TMP_CLOBBERS, "memory");
        if (pass || !__any(mx == 0xffffffffu)) break;
        STAMP(14);
        poll1(ws, fo, tag);
      }
      STAMP(9);
      wait4<16>(sy, 10);
      asm volatile(OPROJ_B_ASM
                   : [d0] "+v"(acc[0]), [d1] "+v"(acc[1]), [d2] "+v"(acc[2]), [d3] "+v"(acc[3]), [d4] "+v"(acc[4])
                   : [b0] "v"(b0), [b1] "v"(b1), [c0] "v"(c0), [c1] "v"(c1), "{v[160:163]}"(o16), "{v[164:167]}"(o17), "{v[168:171]}"(o18), "{v[172:175]}"(o19)
                   : TMP_CLOBBERS, "memory");
      const float og = rbf(1.f - gt);
#pragma unroll
      for (int n = 0; n < 5; n++) { acc[n] *= og; PIN(acc[n]); }
      wait4<16>(sy, 11);
      STAMP(10);
#pragma unroll
      for (int n = 0; n < 5; n++) {
        const u32x2 av = __builtin_bit_cast(u32x2, trd(ata + n * 2560));
        const u32x2 ov = {pkbf(fmaf(gt, bflo(av[0]), acc[n][0]), fmaf(gt, bfhi(av[0]), acc[n][1])),
                          pkbf(fmaf(gt, bflo(av[1]), acc[n][2]), fmaf(gt, bfhi(av[1]), acc[n][3]))};
        *(u32x2*)((char*)out + oo + 32 * n) = ov;
      }
    }
    STAMP(11); CSTAMP(31);
    STAMP_OUT();
    asm volatile(".Lgm_end_main:");
  } else {
    LANE_IDS();
    STAMP_DECL;
    STAMP(0); CSTAMP(28);
    unsigned sy = L_SYNC, me = L_SYNC + 4 * w;
    PIN(sy); PIN(me);
    __builtin_amdgcn_s_setprio(3);
    const int a = w - 4, at = 64 * a + lane;
    const int nch = 3 - (r & 1), ch0 = 80 * g + 5 * (r >> 1) + 3 * (r & 1);
    unsigned in16[10];
    {
      const int d = ch0 + min(a, nch - 1);
      const unsigned e = (d * 128 + lane) * 2, e1 = d * 2;
#define LD16(i, off, p, imm) asm volatile("global_load_ushort %0, %1, %2 offset:" #imm : "=v"(in16[i]) : "v"(off), "s"(p) : "memory")
      LD16(0, e, lar, 0); LD16(1, e, lar, 128); LD16(2, e, aim, 0); LD16(3, e, aim, 128);
      LD16(4, e, crg, 0); LD16(5, e, crg, 128); LD16(6, e, cig, 0); LD16(7, e, cig, 128);
      LD16(8, e1, ldt, 0); LD16(9, e1, dsk, 0);
      const unsigned po = min(((b >> 3) * 16 + c16) * 128u, len - 4);
      asm volatile("s_mov_b32 m0, %2\n\ts_mov_b64 exec, 0xffff\n\tglobal_load_lds_dword %0, %1\n\ts_mov_b64 exec, -1"
                   :: "v"(po & ~3u), "s"(pc), "s"(L_DUMP) : "memory");
    }
    unsigned ids[12], wof[5], wog[5], ga, fa, ad0, ad1, cw, cx, cy, xs, xr, ut;
    const u64 wob = (u64)wo + g * 160;
    const int hb = a & 1, rh = a >> 1;
    unsigned as0, al0, f3o, f5o;
    {
      const float f0 = (float)(2047 - 8 * at);
      const int l0 = 2047 - 8 * at - 128 + (int)(L_KAP / 2) - 0x4280;
#pragma unroll
      for (int k = 0; k < 12; k += 2) {
        const unsigned pb = pkbf(fmaxf(f0 - (float)k, 128.f), fmaxf(f0 - (float)(k + 1), 128.f));
        ids[k] = ((pb & 0xffffu) + min(l0 - k, (int)(L_KAP / 2) - 0x4280)) << 1;
        ids[k + 1] = ((pb >> 16) + min(l0 - k - 1, (int)(L_KAP / 2) - 0x4280)) << 1;
        PIN(ids[k]);
        if (k < 10) PIN(ids[k + 1]);
      }
#pragma unroll
      for (int j = 0; j < 5; j++) {
        const int n = 64 * j + lane, pr = n / 10, i = pr ^ (((pr >> 3) & 1) << 2), cb = (n - pr * 10) * 16;
        wof[j] = (i + 4 * (i >> 4)) * 1280 + cb;
        wog[j] = (20 * (i >> 2) + 16 + (i & 3)) * 1280 + cb;
        PIN(wof[j]); PIN(wog[j]);
      }
      const int x0 = 2031 - c16 + 8 * g4, cc = x0 & 3;
      ga = L_CONV + cc * CPS + (x0 - cc) * 2 - 256 * rh - 192 - 3584; fa = L_U + 512 - 32 + c16 * 32 + g4 * 16 - 256 * rh - 192;
      PIN(ga); PIN(fa);
      ad0 = WS_AT + ch0 * 4096 + (256 * (4 * rh + hb) + 16 * c16 + 4 * g4) * 2; ad1 = ad0 + (hb ? 512 : 1536);
      cw = L_CONV + at * 16; xs = L_XB + a * 2048 + lane * 16; xr = L_XB + (a ^ 2) * 2048 + lane * 16;
      ut = WS_UT + ch0 * 4096 + at * 16; cx = cw + 2 * CPS; cy = cw + 3 * CPS;
      PIN(cx); PIN(cy); PIN(ut); PIN(ad0); PIN(ad1); PIN(cw); PIN(xs); PIN(xr);
      as0 = WS_AT + (80 * g + (at >> 3)) * 4096 + r * 128 + (at & 7) * 16; al0 = L_AT + (at >> 3) * 160 + (at & 7) * 16;
      f3o = WS_F3 + (m32 * 8 + g) * 128; f5o = WS_F5 + (h ? r * 8 + (lane & 7) : m32 * 8 + g) * 128;
      PIN(as0); PIN(al0); PIN(f3o); PIN(f5o);
    }
    const unsigned kg = a < 2 ? L_KG + a * 4096 : L_KG2, kap = L_KAP + a * KAPS;
    unsigned t0 = kap + (g4 >> 1) * 256 * g4 + (c16 << (4 - (g4 >> 1) * (g4 - 1))) + (g4 == 1 ? 8 : 0);
    unsigned t0b = t0 + (g4 == 3 ? 128 : 4), st0 = 256 >> ((g4 >> 1) * (g4 - 1));
    unsigned t1 = g4 == 0 ? kap + 1024 + 2 * c16 : (lane == 16 ? kap + 1280 : kg), st1 = g4 == 0 ? 64 : 0;
    unsigned w0 = kg + lane * 32 + (lane & 4) * 4, rd = kg + (4 * g4 + q4) * 32 + (p4 ^ (2 * (g4 & 1))) * 8;
    PIN(t0); PIN(t0b); PIN(st0); PIN(t1); PIN(st1); PIN(w0); PIN(rd);
#define NLATE "1"
    asm volatile("s_waitcnt vmcnt(" NLATE ")" : "+v"(in16[0]), "+v"(in16[1]), "+v"(in16[2]), "+v"(in16[3]), "+v"(in16[4]), "+v"(in16[5]),
                 "+v"(in16[6]), "+v"(in16[7]), "+v"(in16[8]), "+v"(in16[9]) :: "memory");
    STAMP(22);
    if (a < 3) {
      __builtin_amdgcn_s_setprio(0);
      if (a < nch) {
        const float lr[2] = {bflo(in16[0]), bflo(in16[1])}, ai[2] = {bflo(in16[2]), bflo(in16[3])};
        const float cr[2] = {bflo(in16[4]), bflo(in16[5])}, ci[2] = {bflo(in16[6]), bflo(in16[7])};
        constexpr float LOG2E = 1.4426950408889634f;
        const float dt = rbf(__builtin_amdgcn_exp2f(bflo(in16[8]) * LOG2E));
        unsigned cm = max(max(in16[4] & 0x7fffu, in16[5] & 0x7fffu), max(in16[6] & 0x7fffu, in16[7] & 0x7fffu));
        cm = max(cm, (unsigned)__builtin_amdgcn_update_dpp(0, (int)cm, 0x128, 15, 15, false));
        cm = max(cm, (unsigned)__builtin_amdgcn_update_dpp(0, (int)cm, 0x124, 15, 15, false));
        cm = max(cm, (unsigned)__builtin_amdgcn_update_dpp(0, (int)cm, 0x122, 15, 15, false));
        cm = max(cm, (unsigned)__builtin_amdgcn_update_dpp(0, (int)cm, 0x121, 15, 15, false));
        const auto c1 = __builtin_amdgcn_permlane16_swap(cm, cm, false, false);
        cm = max(c1[0], c1[1]);
        const auto c2 = __builtin_amdgcn_permlane32_swap(cm, cm, false, false);
        const unsigned ce = min(max(max(c2[0], c2[1]) >> 7, 1u), 253u);
        const float cs = __uint_as_float((254u - ce) << 23), ics = __uint_as_float(ce << 23);
        STAMP(10);
        u32x4 PK[2][14];
#pragma unroll
        for (int ps = 0; ps < 2; ps++) {
          const float al = -rbf(__builtin_amdgcn_exp2f(lr[ps] * LOG2E)) * dt * LOG2E, be = ai[ps] * dt * 0.15915494309189535f;
          const f32x2 z1 = zpow(al, -be, 1.f), z8 = zpow(al, be, 8.f), z16 = zpow(al, be, 16.f), z256 = zpow(al, -be, 256.f);
          const f32x2 x0 = {cr[ps] * cs, ci[ps] * -cs}, one = {1.f, 0.f};
#define CHAIN_IN : "{v[128:129]}"(z1), "{v[130:131]}"(z8), "{v[132:133]}"(z16), "{v[134:135]}"(z256), "{v[136:137]}"(x0), "{v[138:139]}"(one) : CHAIN_CLOBBERS
          if (ps == 0) asm volatile(CHAIN0_ASM : CHAIN0_OUT CHAIN_IN); else asm volatile(CHAIN1_ASM : CHAIN1_OUT CHAIN_IN);
        }
        STAMP(16);
        const unsigned w1 = w0 ^ 16;
        f32x4 D[2][2];
#pragma unroll
        for (int i = 0; i < 4; i++) D[i >> 1][i & 1] = f32x4{0, 0, 0, 0};
#pragma unroll
        for (int ps = 0; ps < 2; ps++) {
          u32x4 Y[2][4];
#pragma unroll
          for (int t = 0; t < 4; t++) {
            if (t < 3) {
              lwr<u32x4>(w0, PK[ps][4 * t]); lwr<u32x4>(w1, PK[ps][4 * t + 1]);
              lwr<u32x4>(w0 + 2048, PK[ps][4 * t + 2]); lwr<u32x4>(w1 + 2048, PK[ps][4 * t + 3]);
            } else {
              lwr<u32x4>(w0, PK[ps][12]); lwr<u32x4>(w0 + 2048, PK[ps][13]);
            }
            FENCE();
#pragma unroll
            for (int s = 0; s < 4; s++) {
              const u32x4 f = cat(trd(rd + s * 1024), trd(rd + s * 1024 + 512));
              if (t < 2) Y[t][s] = f;
              else {
                D[t - 2][0] = __builtin_amdgcn_mfma_f32_16x16x32_f16(ash(f), ash(Y[0][s]), D[t - 2][0], 0, 0, 0);
                D[t - 2][1] = __builtin_amdgcn_mfma_f32_16x16x32_f16(ash(f), ash(Y[1][s]), D[t - 2][1], 0, 0, 0);
              }
            }
            FENCE();
          }
        }
        __builtin_amdgcn_s_setprio(3);
        STAMP(11);
        const float ks = 2.f * dt * ics;
        D[0][0][0] = D[0][0][0] * ks + (lane == 0 ? bflo(in16[9]) : 0.f);
        D[0][1][0] *= ks;
#pragma unroll
        for (int yh = 1; yh >= 0; yh--) {
          lwr<unsigned>(t0 + yh * st0, pkbf(D[0][yh][0], D[0][yh][1] * ks));
          lwr<unsigned>(t0b + yh * st0, pkbf(D[0][yh][2] * ks, D[0][yh][3] * ks));
          const unsigned u01 = pkbf(D[1][yh][0] * ks, D[1][yh][1] * ks), u23 = pkbf(D[1][yh][2] * ks, D[1][yh][3] * ks);
          const unsigned tb = t1 + yh * (st1 >> 1);
          lwr<u16>(tb + 3 * st1, (u16)(u23 >> 16)); lwr<u16>(tb + 2 * st1, (u16)u23); lwr<u16>(tb + st1, (u16)(u01 >> 16));
          FENCE();
          lwr<u16>(tb, (u16)u01);
          FENCE();
        }
        lwr<u32x2>(kap - 8, u32x2{0, 0});
      }
      sig<1>(me);
      STAMP(1);
    } else {
      __builtin_amdgcn_s_setprio(0);
      TAG_LOAD(); TAG_WAIT();
      if (lane == 0) lwr<u64>(L_TAG, tag);
      sig<1>(me);
      unsigned wv5[5], xa[5], xc[5], n = SPIN_MAX;
#pragma unroll
      for (int j = 0; j < 5; j++) {
        const int m = 64 * j + lane, row = m / 10;
        wv5[j] = ((row & ~7) + ((row >> 1) & 3) + 4 * (row & 1)) * 1280 + (m - row * 10) * 16;
        const int col = 10 * g + 2 * j + (p4 >> 1), t0 = 8 * g4 + q4;
        xa[j] = L_X + t0 * 1280 + ((col ^ (t0 & 15)) << 4) + (p4 & 1) * 8;
        xc[j] = L_X + (t0 + 4) * 1280 + ((col ^ ((t0 + 4) & 15)) << 4) + (p4 & 1) * 8;
      }
      unsigned ud = WS_UT + (80 * g + c16) * 4096 + r * 128 + g4 * 16;
      unsigned f1o[4];
#pragma unroll
      for (int k = 0; k < 4; k++) f1o[k] = WS_F1 + ((32 * (b & 7) + 32 + 64 * k + lane) & 255) * 128;
      const unsigned wl0 = 327680 - g * 160 + ((b >> 3) * 120 + lane) * 128, wl1 = wl0 + min(64, 119 - lane) * 128;
      unsigned mo = WS_O + 4 * r * 20480 + g * 2048 + lane * 16, mr = WS_O + (4 * r + h) * 20480 + 16384 + g * 512 + m32 * 16;
      const unsigned ma = WS_AT + ch0 * 4096 + lane * 16;
      u64 wqb = (u64)wq + g * 160, f4[4];
      asm volatile(LOADER_ASM
                   : "+{s[22:23]}"(wqb), [n] "+s"(n), [ud] "+v"(ud), [r0] "=&v"(f4[0]), [r1] "=&v"(f4[1]), [r2] "=&v"(f4[2]), [r3] "=&v"(f4[3]) ST_OP
                   : [mp] "v"(sy), [lb] "s"(L_RING), [v0] "v"(wv5[0]), [v1] "v"(wv5[1]), [v2] "v"(wv5[2]), [v3] "v"(wv5[3]), [v4] "v"(wv5[4]),
                     [w0] "v"(wl0), [w1] "v"(wl1), [kvs] "v"(L_KVO + lane * 16), [kvd] "v"(WS_KV + r * 5120 + (g < 3 ? g * 1024 : 2560 + (g - 3) * 512) + lane * 16),
                     [kvm] "s"(g < 2 ? ~0ull : 0xffffffffull), [ws] "s"(ws), [tag] "s"(tag), [tagv] "v"(tag4(tag)), [f1] "v"(WS_F1 + id * 128 + lane * 16), [f2] "v"(WS_F2 + id * 128 + lane * 16),
                     [q0] "v"(f1o[0]), [q1] "v"(f1o[1]), [q2] "v"(f1o[2]), [q3] "v"(f1o[3]),
                     [xa0] "v"(xa[0]), [xa1] "v"(xa[1]), [xa2] "v"(xa[2]), [xa3] "v"(xa[3]), [xa4] "v"(xa[4]),
                     [xc0] "v"(xc[0]), [xc1] "v"(xc[1]), [xc2] "v"(xc[2]), [xc3] "v"(xc[3]), [xc4] "v"(xc[4])
                   : TMP_CLOBBERS, "s24", "s25", "s26", "s27", "s36", "s37", "s38", "s39", "s40", "s41", "s42", "s43", "vcc", "scc", "memory");
      STAMP(2);
      if (b == 0) flag_set(ws, lane * 16, tag4(tag));
      asm volatile(MARKS_ASM : [mo] "+v"(mo), [mr] "+v"(mr)
                   : [ma0] "v"(ma), [ma1] "v"(ma + 4096), [ma2] "v"(ma + (nch - 1) * 4096), [ws] "s"(ws) : TMP_CLOBBERS, "memory");
      STAMP(3);
      poll1(ws, WS_F2 + (m32 * 8 + g) * 128, tag);
      flag_set(ws, WS_F5 + id * 128 + lane * 16, tag4(tag));
      __builtin_amdgcn_s_setprio(0);
    }
    __builtin_amdgcn_s_setprio(0);
    wait4<16>(sy, 1);
    STAMP(4);
    {
      const u32x2 tg = lrd<u32x2>(L_TAG);
      tag = (u64)(unsigned)__builtin_amdgcn_readfirstlane(tg[0]) | ((u64)(unsigned)__builtin_amdgcn_readfirstlane(tg[1]) << 32);
    }
    sig<2>(me);
    wait4<16>(sy, 2);
    u32x4 ur[3];
#pragma unroll
    for (int c = 0; c < 3; c++) ur[c] = __builtin_amdgcn_raw_buffer_load_b128(wsr, ut, min(c, nch - 1) * 4096, SC1);
    wait4<0>(sy, 15);
    PIN(ur[0]); PIN(ur[1]); PIN(ur[2]);
    STAMP(5);
    if (at < 32) lwr<u32x4>(L_U + at * 16, u32x4{0, 0, 0, 0});
    if (at < 8) lwr<u32x4>(L_CONV + (at >> 1) * CPS + 4096 + (at & 1) * 16, u32x4{0, 0, 0, 0});
    __builtin_amdgcn_s_setprio(1);
    unsigned lev = 3;
    PIN(lev);
#pragma unroll 1
    for (int c = 0; c < nch; c++) {
#define BUILD(C) asm volatile(BUILD_ASM :: [i0] "v"(ids[0]), [i1] "v"(ids[1]), [i2] "v"(ids[2]), [i3] "v"(ids[3]), [i4] "v"(ids[4]),       \
        [i5] "v"(ids[5]), [i6] "v"(ids[6]), [i7] "v"(ids[7]), [i8] "v"(ids[8]), [i9] "v"(ids[9]), [i10] "v"(ids[10]), [kap] "n"(C * KAPS), \
        [cw] "v"(cw), [cx] "v"(cx), [cy] "v"(cy), [u] "v"(ur[C]) : CONV_CLOBBERS, "memory")
      if (c == 0) BUILD(0); else if (c == 1) BUILD(1); else BUILD(2);
      if (c == 2) STAMP(23);
      sigv(me, lev);
      wait4<16>(sy, 3 + 2 * c);
      if (c == 2) STAMP(24);
      f32x4 d[4];
#define CONV_OPS : [d0] "=&v"(d[0]), [d1] "=&v"(d[1]), [d2] "=&v"(d[2]), [d3] "=&v"(d[3])  \
      : [ga] "v"(ga), [fa] "v"(fa), [w0] "v"(wof[0]), [w1] "v"(wof[1]), [w2] "v"(wof[2]), [w3] "v"(wof[3]), [w4] "v"(wof[4]),        \
        [wb0] "s"(wob + a * 51200), [wb1] "s"(wob + (a + 4) * 51200), [wb2] "s"(wob + (a + 8) * 51200), [wb3] "s"(wob + (a + 12) * 51200),       \
        [m0] "s"(L_WO + a * 5120), [m1] "s"(L_WO + (a + 4) * 5120), [m2] "s"(L_WO + (a + 8) * 5120), [m3] "s"(L_WO + (a + 12) * 5120)            \
      : CONV_CLOBBERS, "scc", "memory"
#define CONV_OPS2 : [d0] "=&v"(d[0]), [d1] "=&v"(d[1]), [d2] "=&v"(d[2]), [d3] "=&v"(d[3]) : [ga] "v"(ga), [fa] "v"(fa) : CONV_CLOBBERS, "memory"
      if (c == 0) { if (hb == 0) asm volatile(CONV0D_ASM CONV_OPS); else asm volatile(CONV1D_ASM CONV_OPS); }
      else { if (hb == 0) asm volatile(CONV0_ASM CONV_OPS2); else asm volatile(CONV1_ASM CONV_OPS2); }
      if (c == 2) STAMP(25);
      if (rh) { lwr<f32x4>(xs, d[0]); lwr<f32x4>(xs + 1024, d[1]); d[0] = d[2]; d[1] = d[3]; }
      else { lwr<f32x4>(xs, d[2]); lwr<f32x4>(xs + 1024, d[3]); }
      if (c == 0) { VMWAIT(0); if (a == 3) poll1(ws, f5o, tag); }
      sigv(me, lev + 1);
      lev += 2;
      wait4<16>(sy, 4 + 2 * c);
      if (c == 2) STAMP(26);
      const f32x4 e0 = d[0] + lrd<f32x4>(xr), e1 = d[1] + lrd<f32x4>(xr + 1024);
      __builtin_amdgcn_raw_buffer_store_b64(u32x2{min(pkbf(e0[0], e0[1]), 0xfffffffeu), pkbf(e0[2], e0[3])}, wsr, ad0, c * 4096, SC1);
      __builtin_amdgcn_raw_buffer_store_b64(u32x2{min(pkbf(e1[0], e1[1]), 0xfffffffeu), pkbf(e1[2], e1[3])}, wsr, ad1, c * 4096, SC1);
      STAMP(13 + c);
    }
    STAMP(6);
    sig<9>(me);
    wait4<16>(sy, 9);
    if (a == 0) flag_set(ws, WS_F3 + id * 128 + lane * 16, tag4(tag));
#pragma unroll
    for (int j = 0; j < 5; j++) dma16(wog[j], wob + a * 204800, (a == 3 ? L_WOD : L_WO + (16 + a) * 5120) + j * 1024);
    VMWAIT(0);
    STAMP(27);
    sig<10>(me);
    poll1(ws, f3o, tag);
    STAMP(7);
    {
      u32x4 v0, v1, v2;
#pragma unroll 1
      for (unsigned i = 0; i < SPIN_MAX; i++) {
        v0 = __builtin_amdgcn_raw_buffer_load_b128(wsr, as0, 0, SC1);
        v1 = __builtin_amdgcn_raw_buffer_load_b128(wsr, as0, 131072, SC1);
        v2 = __builtin_amdgcn_raw_buffer_load_b128(wsr, as0, a < 2 ? 262144 : 0, SC1);
        const unsigned mx = max(max(max(v0[0], v0[2]), max(v1[0], v1[2])), max(v2[0], v2[2]));
        if (!__any(mx == 0xffffffffu)) break;
      }
      lwr<u32x4>(al0, v0); lwr<u32x4>(al0 + 5120, v1);
      if (a < 2) lwr<u32x4>(al0 + 10240, v2);
    }
    sig<11>(me);
    STAMP(8);
    STAMP_OUT();
    asm volatile(".Lgm_end_aux:");
  }
}

extern "C" void kernel_launch(void* const* d_in, const int* in_sizes, int n_in, void* d_out, int out_size, void* d_ws, size_t ws_size,
                              hipStream_t stream) {
  (void)hipFuncSetAttribute((const void*)gated_mix_kernel, hipFuncAttributeMaxDynamicSharedMemorySize, LDS_BYTES);
  gated_mix_kernel<<<256, 512, LDS_BYTES, stream>>>(
      (const u16*)d_in[0], (const u16*)d_in[1], (const u16*)d_in[2], (const u16*)d_in[3], (const u16*)d_in[4], (const u16*)d_in[5],
      (const u16*)d_in[6], (const u16*)d_in[8], (const u16*)d_in[9], (const u16*)d_in[10], (const u16*)d_in[11], (const u16*)d_in[12],
      (u16*)d_out, (char*)d_ws);
}
```
